```python
import torch

def _softmax(x, axis=-1):
    return torch.softmax(x, dim=axis)

def _gelu(x):
    return torch.nn.functional.gelu(x, approximate='tanh')

def _silu(x):
    return torch.nn.functional.silu(x)

def _sigmoid(x):
    return torch.sigmoid(x)

def _layernorm(x, axis=-1, eps=1e-05):
    m = x.mean(dim=axis, keepdim=True)
    v = x.var(dim=axis, keepdim=True, unbiased=False)
    return (x - m) / torch.sqrt(v + eps)

def _scatter_add(tgt, idx, src, axis=0):
    return tgt.clone().index_add_(axis, idx, src)

def _segment_sum(src, seg, n):
    out = torch.zeros((n,) + src.shape[1:], dtype=src.dtype)
    return out.index_add_(0, seg, src)

def _pad(x, pw):
    flat = tuple((v for pair in reversed(pw) for v in pair))
    return torch.nn.functional.pad(x, flat)
kernel_name = 'gated_mix__memory_aug_recurrent_mem_tr__glu_gated_swiglu__prompt'

def setup_inputs():

    _w = lambda a: (lambda t: t.to(torch.bfloat16) if t.is_floating_point() else t)(torch.as_tensor(a))

    def _A_setup():
        B, S, Dm = (1, 2048, 512)
        H, Dh = (32, 128)
        M, seg = (8, 32)
        seg = min(seg, S)
        S = S // seg * seg
        return {'x': _w((lambda *s: torch.randn(*s, dtype=torch.bfloat16) * 0.05)(B, S, Dm)), 'mem0': _w((lambda *s: torch.randn(*s, dtype=torch.bfloat16) * 0.4)(B, M, Dm)), 'Wq': _w((lambda *s: torch.randn(*s, dtype=torch.bfloat16) * 6.4)(Dm, H, Dh) * Dm ** (-0.5)), 'Wk': _w((lambda *s: torch.randn(*s, dtype=torch.bfloat16) * 6.4)(Dm, H, Dh) * Dm ** (-0.5)), 'Wv': _w((lambda *s: torch.randn(*s, dtype=torch.bfloat16) * 0.4)(Dm, H, Dh) * Dm ** (-0.5)), 'Wo': _w((lambda *s: torch.randn(*s, dtype=torch.bfloat16) * 0.4)(H, Dh, Dm) * (H * Dh) ** (-0.5)), 'seg_len': seg}
    _A_in = _A_setup()

    def _B_setup():
        B, S, D, F = (1, 2048, 512, 1792)
        return {'x': _w((lambda *s: torch.randn(*s, dtype=torch.bfloat16) * 0.05)(B, S, D)), 'w1': _w((lambda *s: torch.randn(*s, dtype=torch.bfloat16) * 0.8)(D, F) * (2.0 / D) ** 0.5), 'w3': _w((lambda *s: torch.randn(*s, dtype=torch.bfloat16) * 0.8)(D, F) * (2.0 / D) ** 0.5), 'w2': _w((lambda *s: torch.randn(*s, dtype=torch.bfloat16) * 0.8)(F, D) * (2.0 / F) ** 0.5)}
    _B_in = _B_setup()
    return {'A_x': _A_in['x'], 'A_mem0': _A_in['mem0'], 'A_Wq': _A_in['Wq'], 'A_Wk': _A_in['Wk'], 'A_Wv': _A_in['Wv'], 'A_Wo': _A_in['Wo'], 'A_seg_len': _A_in['seg_len'], 'B_x': _B_in['x'], 'B_w1': _B_in['w1'], 'B_w3': _B_in['w3'], 'B_w2': _B_in['w2'], 'gate_w': _w((lambda *s: torch.randn(*s, dtype=torch.bfloat16) * 0.05)(512) * 16.0)}

def reference(A_x, A_mem0, A_Wq, A_Wk, A_Wv, A_Wo, A_seg_len, B_x, B_w1, B_w3, B_w2, gate_w):

    def _A_ref(x, mem0, Wq, Wk, Wv, Wo, seg_len):
        B, S, Dm = x.shape
        H, Dh = (Wq.shape[1], Wq.shape[2])
        M = mem0.shape[1]
        scale = Dh ** (-0.5)
        n_seg = S // seg_len
        L = M + seg_len
        idx = torch.arange(L)
        mask = (idx[:, None] < M) | (idx[:, None] >= idx[None, :])
        mem = mem0
        outs = []
        for s in range(n_seg):
            xs = x[:, s * seg_len:(s + 1) * seg_len, :].clone()
            h = torch.concatenate([mem, xs], axis=1)
            q = torch.einsum('btd,dhe->bhte', h, Wq)
            k = torch.einsum('btd,dhe->bhte', h, Wk)
            v = torch.einsum('btd,dhe->bhte', h, Wv)
            scores = torch.einsum('bhqe,bhke->bhqk', q, k) * scale
            scores = torch.where(mask[None, None], scores, -1000000000.0)
            p = _softmax(scores, axis=-1)
            o = torch.einsum('bhqk,bhke->bhqe', p, v)
            o = torch.einsum('bhte,hed->btd', o, Wo)
            mem = o[:, :M, :].clone()
            outs.append(o[:, M:, :])
        return torch.concatenate(outs, axis=1)

    def _B_ref(x, w1, w3, w2):
        gate = torch.einsum('bsd,df->bsf', x, w1)
        up = torch.einsum('bsd,df->bsf', x, w3)
        h = _silu(gate) * up
        out = torch.einsum('bsf,fd->bsd', h, w2)
        return out
    x = A_x
    g = _sigmoid((x * gate_w).sum(-1, keepdims=True))
    a = _A_ref(x, A_mem0, A_Wq, A_Wk, A_Wv, A_Wo, A_seg_len)
    b = _B_ref(x, B_w1, B_w3, B_w2)
    return g * a + (1 - g) * b
```

```cpp
#include <hip/hip_runtime.h>
#include <hip/hip_bf16.h>
typedef unsigned short u16;
__device__ __forceinline__ float b2f(u16 v) { return __uint_as_float((unsigned)v << 16); }
__device__ __forceinline__ u16 f2b(float f) { unsigned u = __float_as_uint(f); u += 0x7fff + ((u >> 16) & 1); return (u16)(u >> 16); }
constexpr int S = 2048, D = 512, H = 32, E = 128, M = 8, SEG = 32, F = 1792, NSEG = 64, L = 40;

__global__ void gemm_naive(const u16* A, int lda, const u16* B, int ldb, u16* C, int ldc, int Mr, int N, int K) {
  __shared__ float As[16][33], Bs[32][17];
  int n = blockIdx.x * 16 + threadIdx.x, m = blockIdx.y * 16 + threadIdx.y;
  float acc = 0;
  for (int k0 = 0; k0 < K; k0 += 32) {
    int t = threadIdx.y * 16 + threadIdx.x;
    for (int i = t; i < 16 * 32; i += 256) { int r = i / 32, c = i % 32; int mm = blockIdx.y * 16 + r; As[r][c] = mm < Mr ? b2f(A[(size_t)mm * lda + k0 + c]) : 0.f; }
    for (int i = t; i < 32 * 16; i += 256) { int r = i / 16, c = i % 16; Bs[r][c] = b2f(B[(size_t)(k0 + r) * ldb + blockIdx.x * 16 + c]); }
    __syncthreads();
    for (int k = 0; k < 32; ++k) acc += As[threadIdx.y][k] * Bs[k][threadIdx.x];
    __syncthreads();
  }
  if (m < Mr) C[(size_t)m * ldc + n] = f2b(acc);
}
__global__ void __launch_bounds__(256) attn_naive(const u16* tok, const u16* memqkv, u16* o, int seg0, int mode) {
  int seg = seg0 + blockIdx.y, h = blockIdx.x;
  const u16* mq = memqkv + (mode == 1 ? (size_t)seg * M * 3 * 4096 : 0);
  __shared__ float k[L][E + 1], v[L][E + 1], q[SEG][E + 1], p[SEG][L + 1];
  int t = threadIdx.x;
  for (int i = t; i < L * E; i += blockDim.x) { int r = i / E, e = i % E;
    const u16* row = r < M ? mq + (size_t)r * 3 * 4096 : tok + (size_t)(seg * SEG + r - M) * 3 * 4096;
    k[r][e] = b2f(row[4096 + h * E + e]); v[r][e] = b2f(row[8192 + h * E + e]); }
  int nq = mode == 0 ? M : SEG;
  for (int i = t; i < nq * E; i += blockDim.x) { int r = i / E, e = i % E;
    const u16* row = mode == 0 ? mq + (size_t)r * 3 * 4096 : tok + (size_t)(seg * SEG + r) * 3 * 4096;
    q[r][e] = b2f(row[h * E + e]); }
  __syncthreads();
  const float scale = 0.08838834764831845f;
  for (int i = t; i < nq * L; i += blockDim.x) { int r = i / L, j = i % L; float s = 0;
#pragma unroll 4
 for (int e = 0; e < E; ++e) s += q[r][e] * k[j][e];
    s = b2f(f2b(b2f(f2b(s)) * b2f(f2b(scale))));
    int qi = mode == 0 ? r : r + M; bool ok = qi < M || qi >= j; p[r][j] = ok ? s : -1e9f; }
  __syncthreads();
  for (int r = t; r < nq; r += blockDim.x) { float mx = -3e38f;
#pragma unroll 1
 for (int j = 0; j < L; ++j) mx = fmaxf(mx, p[r][j]); float sum = 0;
#pragma unroll 1
 for (int j = 0; j < L; ++j) { p[r][j] = __expf(p[r][j] - mx); sum += p[r][j]; }
#pragma unroll 1
    for (int j = 0; j < L; ++j) p[r][j] = b2f(f2b(p[r][j] / sum)); }
  __syncthreads();
  for (int i = t; i < nq * E; i += blockDim.x) { int r = i / E, e = i % E; float s = 0;
#pragma unroll 4
 for (int j = 0; j < L; ++j) s += p[r][j] * v[j][e];
    if (mode == 0) o[(size_t)r * 4096 + h * E + e] = f2b(s); else o[(size_t)(seg * SEG + r) * 4096 + h * E + e] = f2b(s); }
}
__global__ void concat_w(const u16* wq, const u16* wk, const u16* wv, u16* w) {
  size_t i = (size_t)blockIdx.x * 256 + threadIdx.x; if (i >= (size_t)D * 4096) return; int d = i / 4096, c = i % 4096;
  w[(size_t)d * 12288 + c] = wq[i]; w[(size_t)d * 12288 + 4096 + c] = wk[i]; w[(size_t)d * 12288 + 8192 + c] = wv[i];
}
__global__ void silu_mul(const u16* g, const u16* u, u16* h, size_t n) { size_t i = (size_t)blockIdx.x * 256 + threadIdx.x; if (i >= n) return; float x = b2f(g[i]); float s = b2f(f2b(x / (1.f + expf(-x)))); h[i] = f2b(s * b2f(u[i])); }
__global__ void combine(const u16* x, const u16* gw, const u16* a, const u16* b, u16* out) {
  int m = blockIdx.x; __shared__ float red[256]; float s = 0; for (int d = threadIdx.x; d < D; d += 256) s += b2f(f2b(b2f(x[(size_t)m * D + d]) * b2f(gw[d]))); red[threadIdx.x] = s; __syncthreads();
  for (int o = 128; o > 0; o >>= 1) { if (threadIdx.x < o) red[threadIdx.x] += red[threadIdx.x + o]; __syncthreads(); }
  float z = b2f(f2b(red[0])); float g = b2f(f2b(1.f / (1.f + expf(-z)))); float g1 = b2f(f2b(1.f - g));
  for (int d = threadIdx.x; d < D; d += 256) { float r = b2f(f2b(g * b2f(a[(size_t)m * D + d]))) + b2f(f2b(g1 * b2f(b[(size_t)m * D + d]))); out[(size_t)m * D + d] = f2b(r); }
}
extern "C" void kernel_launch(void* const* d_in, const int* in_sizes, int n_in, void* d_out, int out_size, void* d_ws, size_t ws_size, hipStream_t stream) {
  const u16* x = (const u16*)d_in[0]; const u16* mem0 = (const u16*)d_in[1]; const u16* wq = (const u16*)d_in[2]; const u16* wk = (const u16*)d_in[3]; const u16* wv = (const u16*)d_in[4]; const u16* wo = (const u16*)d_in[5];
  const u16* w1 = (const u16*)d_in[8]; const u16* w3 = (const u16*)d_in[9]; const u16* w2 = (const u16*)d_in[10]; const u16* gw = (const u16*)d_in[11];
  char* ws = (char*)d_ws; size_t o = 0; auto take = [&](size_t b) { char* p = ws + o; o += (b + 255) & ~(size_t)255; return (u16*)p; };
  u16* wqkv = take((size_t)D * 12288 * 2); u16* tok = take((size_t)S * 12288 * 2); u16* memq = take((size_t)NSEG * M * 12288 * 2); u16* mem = take((size_t)(NSEG + 1) * M * D * 2);
  u16* om = take((size_t)M * 4096 * 2); u16* ox = take((size_t)S * 4096 * 2); u16* a = take((size_t)S * D * 2); u16* gt = take((size_t)S * F * 2); u16* up = take((size_t)S * F * 2); u16* hh = take((size_t)S * F * 2); u16* b = take((size_t)S * D * 2);
  dim3 tb(16, 16);
  concat_w<<<D * 4096 / 256, 256, 0, stream>>>(wq, wk, wv, wqkv);
  gemm_naive<<<dim3(12288 / 16, S / 16), tb, 0, stream>>>(x, D, wqkv, 12288, tok, 12288, S, 12288, D);
  hipMemcpyAsync(mem, mem0, M * D * 2, hipMemcpyDeviceToDevice, stream);
  for (int s = 0; s < NSEG; ++s) {
    gemm_naive<<<dim3(12288 / 16, 1), tb, 0, stream>>>(mem + (size_t)s * M * D, D, wqkv, 12288, memq + (size_t)s * M * 12288, 12288, M, 12288, D);
    if (s + 1 < NSEG) {
      attn_naive<<<dim3(H, 1), 256, 0, stream>>>(tok, memq + (size_t)s * M * 12288, om, s, 0);
      gemm_naive<<<dim3(D / 16, 1), tb, 0, stream>>>(om, 4096, wo, D, mem + (size_t)(s + 1) * M * D, D, M, D, 4096);
    }
  }
  attn_naive<<<dim3(H, NSEG), 256, 0, stream>>>(tok, memq, ox, 0, 1);
  gemm_naive<<<dim3(D / 16, S / 16), tb, 0, stream>>>(ox, 4096, wo, D, a, D, S, D, 4096);
  gemm_naive<<<dim3(F / 16, S / 16), tb, 0, stream>>>(x, D, w1, F, gt, F, S, F, D);
  gemm_naive<<<dim3(F / 16, S / 16), tb, 0, stream>>>(x, D, w3, F, up, F, S, F, D);
  silu_mul<<<(S * F + 255) / 256, 256, 0, stream>>>(gt, up, hh, (size_t)S * F);
  gemm_naive<<<dim3(D / 16, S / 16), tb, 0, stream>>>(hh, F, w2, D, b, D, S, D, F);
  combine<<<S, 256, 0, stream>>>(x, gw, a, b, (u16*)d_out);
}
```

```cpp
#include <hip/hip_runtime.h>
#include <hip/hip_bf16.h>
#include <stdint.h>

typedef unsigned short u16;
typedef unsigned v4u __attribute__((ext_vector_type(4)));
typedef unsigned v2u __attribute__((ext_vector_type(2)));
typedef float f32x4 __attribute__((ext_vector_type(4)));
typedef __bf16 bf16x8 __attribute__((ext_vector_type(8)));
typedef __bf16 bf16x4 __attribute__((ext_vector_type(4)));
typedef __bf16 bf16x2 __attribute__((ext_vector_type(2)));
typedef short s16x4 __attribute__((ext_vector_type(4)));
#define GAS __attribute__((address_space(1)))
#define LAS __attribute__((address_space(3)))
#define RLX_AGENT __ATOMIC_RELAXED, __HIP_MEMORY_SCOPE_AGENT

constexpr unsigned MB = 1u << 20;
constexpr unsigned WS_SPAN = 96 * MB;
constexpr unsigned OFF_CTRL = 2 * MB, OFF_KX = 4 * MB, OFF_VX = 20 * MB, OFF_QX = 36 * MB, OFF_OX = 52 * MB, OFF_HB = 68 * MB;
constexpr unsigned OFF_KM = 76 * MB, OFF_VM = 80 * MB, OFF_BB = 84 * MB, OFF_GG = 88 * MB, OFF_DBG = 90 * MB;
constexpr unsigned CT_XCNT = OFF_CTRL, CT_OVF = OFF_CTRL + 8 * 128, CT_CVAL = OFF_CTRL + 9 * 128, CT_ERR = OFF_CTRL + 10 * 128;
constexpr unsigned CT_GCNT = OFF_CTRL + 11 * 128;
constexpr unsigned CT_PROG = OFF_CTRL + 16 * 128;
constexpr unsigned CT_DPROG = OFF_CTRL + 96 * 128;
constexpr unsigned CT_FM = OFF_CTRL + 160 * 128;
constexpr unsigned CT_OCNT = OFF_CTRL + 192 * 128;
constexpr unsigned INIT_BYTES = 2 * MB + 64 * 1024;
constexpr int LDS_BYTES = 163840;
constexpr int SPIN_CAP = 400000;

struct Params {
  const u16 *x, *mem0, *wq, *wk, *wv, *wo, *w1, *w3, *w2, *gw;
  u16* out;
  char* base;
  int d1, d2, mask, pad;
};

__device__ __forceinline__ unsigned xcc_id() { return (unsigned)__builtin_amdgcn_s_getreg(20 | (3 << 11)) & 0xfu; }
__device__ __forceinline__ unsigned rfl(unsigned v) { return __builtin_amdgcn_readfirstlane(v); }
__device__ __forceinline__ unsigned ld_agent(const char* base, unsigned off) { return __hip_atomic_load((GAS unsigned*)(base + off), RLX_AGENT); }
__device__ __forceinline__ void st_agent(char* base, unsigned off, unsigned v) { __hip_atomic_store((GAS unsigned*)(base + off), v, RLX_AGENT); }
__device__ __forceinline__ void add_agent(char* base, unsigned off) { __hip_atomic_fetch_add((GAS unsigned*)(base + off), 1u, RLX_AGENT); }
__device__ __forceinline__ void wait_vm0() { asm volatile("s_waitcnt vmcnt(0)" ::: "memory"); }
__device__ __forceinline__ void wait_lgkm0() { asm volatile("s_waitcnt lgkmcnt(0)" ::: "memory"); }
__device__ __forceinline__ void bar() { asm volatile("s_barrier" ::: "memory"); }
__device__ __forceinline__ unsigned pk2(float a, float b) { bf16x2 r = {(__bf16)a, (__bf16)b}; return __builtin_bit_cast(unsigned, r); }
__device__ __forceinline__ float b2f(u16 v) { return __uint_as_float((unsigned)v << 16); }
__device__ __forceinline__ v4u make_srd(const void* p, unsigned bytes) {
  const uint64_t a = (uint64_t)p;
  return (v4u){rfl((unsigned)a), rfl((unsigned)(a >> 32)) & 0xffffu, bytes, 0x00020000u};
}
__device__ __forceinline__ __amdgpu_buffer_rsrc_t ws_rsrc(const Params& p) { return __builtin_amdgcn_make_buffer_rsrc(p.base, 0, WS_SPAN, 0x00020000); }
__device__ __forceinline__ v4u ld16(__amdgpu_buffer_rsrc_t r, unsigned voff, unsigned soff) { return __builtin_amdgcn_raw_buffer_load_b128(r, voff, soff, 16); }
__device__ __forceinline__ void st16(__amdgpu_buffer_rsrc_t r, v4u v, unsigned voff, unsigned soff) { __builtin_amdgcn_raw_buffer_store_b128(v, r, voff, soff, 16); }
__device__ __forceinline__ void dma_buf(unsigned lds, unsigned voff, v4u srd, unsigned soff) {
  asm volatile("s_mov_b32 m0, %0\n\ts_nop 0\n\tbuffer_load_dwordx4 %1, %2, %3 offen lds" ::"s"(lds), "v"(voff), "s"(srd), "s"(soff) : "memory");
}
__device__ __forceinline__ void dma_ptr(unsigned lds, const void* ptr) {
  asm volatile("s_mov_b32 m0, %0\n\ts_nop 0\n\tglobal_load_lds_dwordx4 %1, off" ::"s"(lds), "v"(ptr) : "memory");
}
__device__ __forceinline__ v2u lds_tr(unsigned addr) {
  return __builtin_bit_cast(v2u, __builtin_amdgcn_ds_read_tr16_b64_v4bf16((LAS bf16x4*)(size_t)addr));
}
__device__ __forceinline__ v4u lds_ld16(unsigned addr) { return *(LAS v4u*)(size_t)addr; }
__device__ __forceinline__ void lds_st16(unsigned addr, v4u v) { *(LAS v4u*)(size_t)addr = v; }

__device__ __forceinline__ void mfma_wa0(f32x4& d, const v4u& w, const v4u& x) { asm volatile("v_mfma_f32_16x16x32_bf16 %0, %1, %2, 0" : "=&v"(d) : "a"(w), "v"(x)); }
__device__ __forceinline__ void mfma_wa(f32x4& d, const v4u& w, const v4u& x) { asm volatile("v_mfma_f32_16x16x32_bf16 %0, %1, %2, %0" : "+v"(d) : "a"(w), "v"(x)); }
__device__ __forceinline__ void mfma_wb0(f32x4& d, const v4u& w, const v4u& x) { asm volatile("v_mfma_f32_16x16x32_bf16 %0, %2, %1, 0" : "=&v"(d) : "a"(w), "v"(x)); }
__device__ __forceinline__ void mfma_wb(f32x4& d, const v4u& w, const v4u& x) { asm volatile("v_mfma_f32_16x16x32_bf16 %0, %2, %1, %0" : "+v"(d) : "a"(w), "v"(x)); }
__device__ __forceinline__ void mfma_settle(f32x4& a, f32x4& b) { asm volatile("s_nop 7\n\ts_nop 1" : "+v"(a), "+v"(b)); }

__device__ __forceinline__ bool wait_words(const Params& p, unsigned off, int want, int code, int nap) {
  for (int s = 0; s < SPIN_CAP; ++s) {
    asm volatile("" ::: "memory");
    const int v = off != ~0u ? (int)ld_agent(p.base, off) : 0x7fffffff;
    if (__all(v >= want)) return true;
    for (int q = 0; q < nap; ++q) __builtin_amdgcn_s_sleep(2);
  }
  st_agent(p.base, CT_ERR, (unsigned)code);
  return false;
}
__device__ __forceinline__ bool wg_wait(const Params& p, int w, unsigned off, int want, int code, int nap, unsigned flag) {
  if (w == 0) {
    const bool ok = wait_words(p, off, want, code, nap);
    *(volatile LAS unsigned*)(size_t)flag = ok ? 1u : 0u;
    wait_lgkm0();
  }
  bar();
  const unsigned v = *(volatile LAS unsigned*)(size_t)flag;
  wait_lgkm0();
  return rfl(v) != 0;
}

__device__ __forceinline__ unsigned probe_rt(char* p) {
  unsigned best = ~0u, v, z = 0;
  for (int r = 0; r < 3; ++r) {
    const unsigned long long c0 = __builtin_amdgcn_s_memtime();
    asm volatile("global_atomic_add %0, %1, %2, off sc0\n\ts_waitcnt vmcnt(0)" : "=v"(v) : "v"(p), "v"(z) : "memory");
    const unsigned long long c1 = __builtin_amdgcn_s_memtime();
    const unsigned d = (unsigned)(c1 - c0);
    if (r > 0 && d < best) best = d;
  }
  return best;
}

__device__ __forceinline__ void head_role(const Params& p, int h, int g, int w) {
  const uint64_t pb = (uint64_t)p.base;
  const uint64_t pq = (uint64_t)(p.wq + h * 128), pk = (uint64_t)(p.wk + h * 128), pv = (uint64_t)(p.wv + h * 128);
  const uint64_t po = (uint64_t)(p.wo + (size_t)h * 128 * 512 + g * 256), pm = (uint64_t)p.mem0;
  const unsigned kprog = CT_PROG + (16 + (h >> 1)) * 128, vprog = CT_PROG + (32 + (h >> 1)) * 128;
  asm volatile(
"s_mov_b32 s36, %[base_lo]\n"
"s_mov_b32 s37, %[base_hi]\n"
"s_mov_b32 s38, 100663296\n"
"s_mov_b32 s39, 0x00020000\n"
"s_mov_b32 s43, 0x00020000\n"
"s_lshl_b32 s60, %[g], 5\n"
"s_add_u32 s60, s60, %[h]\n"
"v_mbcnt_lo_u32_b32 v100, -1, 0\n"
"v_mbcnt_hi_u32_b32 v100, -1, v100\n"
"v_lshrrev_b32 v101, 4, v100\n"
"v_and_b32 v102, 1, v101\n"
"s_and_b32 s52, %[w], 1\n"
"s_lshl_b32 s52, s52, 1\n"
"v_or_b32 v102, s52, v102\n"
"v_lshlrev_b32 v102, 1, v102\n"
"v_and_b32 v103, 15, v100\n"
"v_xor_b32 v104, v102, v103\n"
"v_lshlrev_b32 v104, 4, v104\n"
"v_lshl_add_u32 v105, v101, 13, v104\n"
"v_xor_b32 v106, 128, v105\n"
"v_lshrrev_b32 v101, 5, v100\n"
"s_and_b32 s52, %[w], 2\n"
"v_or_b32 v102, s52, v101\n"
"v_lshlrev_b32 v102, 1, v102\n"
"v_and_b32 v103, 31, v100\n"
"v_xor_b32 v104, v102, v103\n"
"v_lshlrev_b32 v104, 4, v104\n"
"v_lshl_add_u32 v107, v101, 10, v104\n"
"v_xor_b32 v108, 128, v107\n"
"v_lshrrev_b32 v115, 4, v100\n"
"v_bfe_u32 v116, v100, 2, 2\n"
"v_and_b32 v117, 3, v100\n"
"v_and_b32 v101, 1, v115\n"
"v_lshlrev_b32 v102, 5, v101\n"
"v_lshl_or_b32 v118, v101, 2, v116\n"
"v_lshrrev_b32 v101, 1, v115\n"
"v_lshl_add_u32 v102, v101, 3, v102\n"
"v_lshrrev_b32 v101, 1, v116\n"
"v_lshl_add_u32 v102, v101, 2, v102\n"
"v_and_b32 v101, 1, v116\n"
"v_add_u32 v102, v102, v101\n"
"v_lshlrev_b32 v102, 8, v102\n"
"v_lshrrev_b32 v101, 1, v117\n"
"v_lshl_add_u32 v102, v101, 4, v102\n"
"v_and_b32 v101, 1, v117\n"
"v_lshl_add_u32 v102, v101, 3, v102\n"
"v_lshlrev_b32 v103, 1, v118\n"
"s_lshl_b32 s52, %[w], 2\n"
"s_add_u32 s52, s52, 0\n"
"v_xor_b32 v101, s52, v103\n"
"v_lshl_add_u32 v109, v101, 4, v102\n"
"s_lshl_b32 s52, %[w], 2\n"
"s_add_u32 s52, s52, 2\n"
"v_xor_b32 v101, s52, v103\n"
"v_lshl_add_u32 v110, v101, 4, v102\n"
"v_lshrrev_b32 v101, 1, v115\n"
"v_lshlrev_b32 v102, 4, v101\n"
"v_and_b32 v101, 1, v115\n"
"v_lshl_add_u32 v102, v101, 2, v102\n"
"v_lshlrev_b32 v118, 1, v101\n"
"v_lshrrev_b32 v101, 1, v116\n"
"v_lshl_add_u32 v102, v101, 3, v102\n"
"v_lshl_or_b32 v118, v101, 2, v118\n"
"v_and_b32 v101, 1, v116\n"
"v_add_u32 v102, v102, v101\n"
"v_or_b32 v118, v118, v101\n"
"v_lshlrev_b32 v102, 9, v102\n"
"v_lshrrev_b32 v101, 1, v117\n"
"v_lshl_add_u32 v102, v101, 4, v102\n"
"v_and_b32 v101, 1, v117\n"
"v_lshl_add_u32 v102, v101, 3, v102\n"
"v_add_u32 v102, 32768, v102\n"
"v_lshlrev_b32 v103, 1, v118\n"
"s_lshl_b32 s52, %[w], 3\n"
"s_add_u32 s52, s52, 0\n"
"v_xor_b32 v101, s52, v103\n"
"v_lshl_add_u32 v111, v101, 4, v102\n"
"s_lshl_b32 s52, %[w], 3\n"
"s_add_u32 s52, s52, 2\n"
"v_xor_b32 v101, s52, v103\n"
"v_lshl_add_u32 v112, v101, 4, v102\n"
"s_lshl_b32 s52, %[w], 3\n"
"s_add_u32 s52, s52, 4\n"
"v_xor_b32 v101, s52, v103\n"
"v_lshl_add_u32 v113, v101, 4, v102\n"
"s_lshl_b32 s52, %[w], 3\n"
"s_add_u32 s52, s52, 6\n"
"v_xor_b32 v101, s52, v103\n"
"v_lshl_add_u32 v114, v101, 4, v102\n"
"s_nop 1\n"
"s_lshl_b32 s65, %[w], 15\n"
"s_lshl_b32 s66, %[w], 10\n"
"s_lshl_b32 s67, %[w], 11\n"
"s_mov_b32 s40, %[wq_lo]\n"
"s_mov_b32 s41, %[wq_hi]\n"
"s_mov_b32 s42, 4186368\n"
"s_add_u32 m0, s66, 32768\n"
"s_add_u32 s52, s65, 0\n"
"buffer_load_dwordx4 v105, s[40:43], s52 offen lds\n"
"s_add_u32 m0, s66, 36864\n"
"s_add_u32 s52, s65, 131072\n"
"buffer_load_dwordx4 v105, s[40:43], s52 offen lds\n"
"s_add_u32 m0, s66, 40960\n"
"s_add_u32 s52, s65, 262144\n"
"buffer_load_dwordx4 v106, s[40:43], s52 offen lds\n"
"s_add_u32 m0, s66, 45056\n"
"s_add_u32 s52, s65, 393216\n"
"buffer_load_dwordx4 v106, s[40:43], s52 offen lds\n"
"s_add_u32 m0, s66, 49152\n"
"s_add_u32 s52, s65, 524288\n"
"buffer_load_dwordx4 v105, s[40:43], s52 offen lds\n"
"s_add_u32 m0, s66, 53248\n"
"s_add_u32 s52, s65, 655360\n"
"buffer_load_dwordx4 v105, s[40:43], s52 offen lds\n"
"s_add_u32 m0, s66, 57344\n"
"s_add_u32 s52, s65, 786432\n"
"buffer_load_dwordx4 v106, s[40:43], s52 offen lds\n"
"s_add_u32 m0, s66, 61440\n"
"s_add_u32 s52, s65, 917504\n"
"buffer_load_dwordx4 v106, s[40:43], s52 offen lds\n"
"s_mov_b32 s40, %[wq_lo]\n"
"s_mov_b32 s41, %[wq_hi]\n"
"s_mov_b32 s42, 4186368\n"
"s_add_u32 m0, s66, 65536\n"
"s_add_u32 s52, s65, 1048576\n"
"buffer_load_dwordx4 v105, s[40:43], s52 offen lds\n"
"s_add_u32 m0, s66, 69632\n"
"s_add_u32 s52, s65, 1179648\n"
"buffer_load_dwordx4 v105, s[40:43], s52 offen lds\n"
"s_add_u32 m0, s66, 73728\n"
"s_add_u32 s52, s65, 1310720\n"
"buffer_load_dwordx4 v106, s[40:43], s52 offen lds\n"
"s_add_u32 m0, s66, 77824\n"
"s_add_u32 s52, s65, 1441792\n"
"buffer_load_dwordx4 v106, s[40:43], s52 offen lds\n"
"s_add_u32 m0, s66, 81920\n"
"s_add_u32 s52, s65, 1572864\n"
"buffer_load_dwordx4 v105, s[40:43], s52 offen lds\n"
"s_add_u32 m0, s66, 86016\n"
"s_add_u32 s52, s65, 1703936\n"
"buffer_load_dwordx4 v105, s[40:43], s52 offen lds\n"
"s_add_u32 m0, s66, 90112\n"
"s_add_u32 s52, s65, 1835008\n"
"buffer_load_dwordx4 v106, s[40:43], s52 offen lds\n"
"s_add_u32 m0, s66, 94208\n"
"s_add_u32 s52, s65, 1966080\n"
"buffer_load_dwordx4 v106, s[40:43], s52 offen lds\n"
"s_mov_b32 s40, %[wq_lo]\n"
"s_mov_b32 s41, %[wq_hi]\n"
"s_mov_b32 s42, 4186368\n"
"s_add_u32 m0, s66, 98304\n"
"s_add_u32 s52, s65, 2097152\n"
"buffer_load_dwordx4 v105, s[40:43], s52 offen lds\n"
"s_add_u32 m0, s66, 102400\n"
"s_add_u32 s52, s65, 2228224\n"
"buffer_load_dwordx4 v105, s[40:43], s52 offen lds\n"
"s_add_u32 m0, s66, 106496\n"
"s_add_u32 s52, s65, 2359296\n"
"buffer_load_dwordx4 v106, s[40:43], s52 offen lds\n"
"s_add_u32 m0, s66, 110592\n"
"s_add_u32 s52, s65, 2490368\n"
"buffer_load_dwordx4 v106, s[40:43], s52 offen lds\n"
"s_add_u32 m0, s66, 114688\n"
"s_add_u32 s52, s65, 2621440\n"
"buffer_load_dwordx4 v105, s[40:43], s52 offen lds\n"
"s_add_u32 m0, s66, 118784\n"
"s_add_u32 s52, s65, 2752512\n"
"buffer_load_dwordx4 v105, s[40:43], s52 offen lds\n"
"s_add_u32 m0, s66, 122880\n"
"s_add_u32 s52, s65, 2883584\n"
"buffer_load_dwordx4 v106, s[40:43], s52 offen lds\n"
"s_add_u32 m0, s66, 126976\n"
"s_add_u32 s52, s65, 3014656\n"
"buffer_load_dwordx4 v106, s[40:43], s52 offen lds\n"
"s_waitcnt vmcnt(16)\n"
"s_barrier\n"
"v_add_u32 v119, 32768, v109\n"
"v_add_u32 v120, 32768, v110\n"
"s_mov_b32 s40, %[wq_lo]\n"
"s_mov_b32 s41, %[wq_hi]\n"
"s_mov_b32 s42, 4186368\n"
"ds_read_b64_tr_b16 a[0:1], v119\n"
"ds_read_b64_tr_b16 a[2:3], v119 offset:512\n"
"s_add_u32 m0, s66, 131072\n"
"s_add_u32 s52, s65, 3145728\n"
"buffer_load_dwordx4 v105, s[40:43], s52 offen lds\n"
"ds_read_b64_tr_b16 a[4:5], v119 offset:4096\n"
"ds_read_b64_tr_b16 a[6:7], v119 offset:4608\n"
"s_add_u32 m0, s66, 135168\n"
"s_add_u32 s52, s65, 3276800\n"
"buffer_load_dwordx4 v105, s[40:43], s52 offen lds\n"
"ds_read_b64_tr_b16 a[8:9], v119 offset:16384\n"
"ds_read_b64_tr_b16 a[10:11], v119 offset:16896\n"
"s_add_u32 m0, s66, 139264\n"
"s_add_u32 s52, s65, 3407872\n"
"buffer_load_dwordx4 v106, s[40:43], s52 offen lds\n"
"ds_read_b64_tr_b16 a[12:13], v119 offset:20480\n"
"ds_read_b64_tr_b16 a[14:15], v119 offset:20992\n"
"s_add_u32 m0, s66, 143360\n"
"s_add_u32 s52, s65, 3538944\n"
"buffer_load_dwordx4 v106, s[40:43], s52 offen lds\n"
"ds_read_b64_tr_b16 a[64:65], v120\n"
"ds_read_b64_tr_b16 a[66:67], v120 offset:512\n"
"s_add_u32 m0, s66, 147456\n"
"s_add_u32 s52, s65, 3670016\n"
"buffer_load_dwordx4 v105, s[40:43], s52 offen lds\n"
"ds_read_b64_tr_b16 a[68:69], v120 offset:4096\n"
"ds_read_b64_tr_b16 a[70:71], v120 offset:4608\n"
"s_add_u32 m0, s66, 151552\n"
"s_add_u32 s52, s65, 3801088\n"
"buffer_load_dwordx4 v105, s[40:43], s52 offen lds\n"
"ds_read_b64_tr_b16 a[72:73], v120 offset:16384\n"
"ds_read_b64_tr_b16 a[74:75], v120 offset:16896\n"
"s_add_u32 m0, s66, 155648\n"
"s_add_u32 s52, s65, 3932160\n"
"buffer_load_dwordx4 v106, s[40:43], s52 offen lds\n"
"s_waitcnt lgkmcnt(8)\n"
"ds_read_b64_tr_b16 a[76:77], v120 offset:20480\n"
"ds_read_b64_tr_b16 a[78:79], v120 offset:20992\n"
"s_add_u32 m0, s66, 159744\n"
"s_add_u32 s52, s65, 4063232\n"
"buffer_load_dwordx4 v106, s[40:43], s52 offen lds\n"
"s_waitcnt lgkmcnt(0)\n"
"s_waitcnt vmcnt(16)\n"
"s_barrier\n"
"v_add_u32 v119, 65536, v109\n"
"v_add_u32 v120, 65536, v110\n"
"s_mov_b32 s40, %[wk_lo]\n"
"s_mov_b32 s41, %[wk_hi]\n"
"s_mov_b32 s42, 4186368\n"
"ds_read_b64_tr_b16 a[16:17], v119\n"
"ds_read_b64_tr_b16 a[18:19], v119 offset:512\n"
"s_add_u32 m0, s66, 32768\n"
"s_add_u32 s52, s65, 0\n"
"buffer_load_dwordx4 v105, s[40:43], s52 offen lds\n"
"ds_read_b64_tr_b16 a[20:21], v119 offset:4096\n"
"ds_read_b64_tr_b16 a[22:23], v119 offset:4608\n"
"s_add_u32 m0, s66, 36864\n"
"s_add_u32 s52, s65, 131072\n"
"buffer_load_dwordx4 v105, s[40:43], s52 offen lds\n"
"ds_read_b64_tr_b16 a[24:25], v119 offset:16384\n"
"ds_read_b64_tr_b16 a[26:27], v119 offset:16896\n"
"s_add_u32 m0, s66, 40960\n"
"s_add_u32 s52, s65, 262144\n"
"buffer_load_dwordx4 v106, s[40:43], s52 offen lds\n"
"ds_read_b64_tr_b16 a[28:29], v119 offset:20480\n"
"ds_read_b64_tr_b16 a[30:31], v119 offset:20992\n"
"s_add_u32 m0, s66, 45056\n"
"s_add_u32 s52, s65, 393216\n"
"buffer_load_dwordx4 v106, s[40:43], s52 offen lds\n"
"ds_read_b64_tr_b16 a[80:81], v120\n"
"ds_read_b64_tr_b16 a[82:83], v120 offset:512\n"
"s_add_u32 m0, s66, 49152\n"
"s_add_u32 s52, s65, 524288\n"
"buffer_load_dwordx4 v105, s[40:43], s52 offen lds\n"
"ds_read_b64_tr_b16 a[84:85], v120 offset:4096\n"
"ds_read_b64_tr_b16 a[86:87], v120 offset:4608\n"
"s_add_u32 m0, s66, 53248\n"
"s_add_u32 s52, s65, 655360\n"
"buffer_load_dwordx4 v105, s[40:43], s52 offen lds\n"
"ds_read_b64_tr_b16 a[88:89], v120 offset:16384\n"
"ds_read_b64_tr_b16 a[90:91], v120 offset:16896\n"
"s_add_u32 m0, s66, 57344\n"
"s_add_u32 s52, s65, 786432\n"
"buffer_load_dwordx4 v106, s[40:43], s52 offen lds\n"
"s_waitcnt lgkmcnt(8)\n"
"ds_read_b64_tr_b16 a[92:93], v120 offset:20480\n"
"ds_read_b64_tr_b16 a[94:95], v120 offset:20992\n"
"s_add_u32 m0, s66, 61440\n"
"s_add_u32 s52, s65, 917504\n"
"buffer_load_dwordx4 v106, s[40:43], s52 offen lds\n"
"s_waitcnt lgkmcnt(0)\n"
"s_waitcnt vmcnt(16)\n"
"s_barrier\n"
"v_add_u32 v119, 98304, v109\n"
"v_add_u32 v120, 98304, v110\n"
"s_mov_b32 s40, %[wk_lo]\n"
"s_mov_b32 s41, %[wk_hi]\n"
"s_mov_b32 s42, 4186368\n"
"ds_read_b64_tr_b16 a[32:33], v119\n"
"ds_read_b64_tr_b16 a[34:35], v119 offset:512\n"
"s_add_u32 m0, s66, 65536\n"
"s_add_u32 s52, s65, 1048576\n"
"buffer_load_dwordx4 v105, s[40:43], s52 offen lds\n"
"ds_read_b64_tr_b16 a[36:37], v119 offset:4096\n"
"ds_read_b64_tr_b16 a[38:39], v119 offset:4608\n"
"s_add_u32 m0, s66, 69632\n"
"s_add_u32 s52, s65, 1179648\n"
"buffer_load_dwordx4 v105, s[40:43], s52 offen lds\n"
"ds_read_b64_tr_b16 a[40:41], v119 offset:16384\n"
"ds_read_b64_tr_b16 a[42:43], v119 offset:16896\n"
"s_add_u32 m0, s66, 73728\n"
"s_add_u32 s52, s65, 1310720\n"
"buffer_load_dwordx4 v106, s[40:43], s52 offen lds\n"
"ds_read_b64_tr_b16 a[44:45], v119 offset:20480\n"
"ds_read_b64_tr_b16 a[46:47], v119 offset:20992\n"
"s_add_u32 m0, s66, 77824\n"
"s_add_u32 s52, s65, 1441792\n"
"buffer_load_dwordx4 v106, s[40:43], s52 offen lds\n"
"ds_read_b64_tr_b16 a[96:97], v120\n"
"ds_read_b64_tr_b16 a[98:99], v120 offset:512\n"
"s_add_u32 m0, s66, 81920\n"
"s_add_u32 s52, s65, 1572864\n"
"buffer_load_dwordx4 v105, s[40:43], s52 offen lds\n"
"ds_read_b64_tr_b16 a[100:101], v120 offset:4096\n"
"ds_read_b64_tr_b16 a[102:103], v120 offset:4608\n"
"s_add_u32 m0, s66, 86016\n"
"s_add_u32 s52, s65, 1703936\n"
"buffer_load_dwordx4 v105, s[40:43], s52 offen lds\n"
"ds_read_b64_tr_b16 a[104:105], v120 offset:16384\n"
"ds_read_b64_tr_b16 a[106:107], v120 offset:16896\n"
"s_add_u32 m0, s66, 90112\n"
"s_add_u32 s52, s65, 1835008\n"
"buffer_load_dwordx4 v106, s[40:43], s52 offen lds\n"
"s_waitcnt lgkmcnt(8)\n"
"ds_read_b64_tr_b16 a[108:109], v120 offset:20480\n"
"ds_read_b64_tr_b16 a[110:111], v120 offset:20992\n"
"s_add_u32 m0, s66, 94208\n"
"s_add_u32 s52, s65, 1966080\n"
"buffer_load_dwordx4 v106, s[40:43], s52 offen lds\n"
"s_waitcnt lgkmcnt(0)\n"
"s_waitcnt vmcnt(16)\n"
"s_barrier\n"
"v_add_u32 v119, 131072, v109\n"
"v_add_u32 v120, 131072, v110\n"
"s_mov_b32 s40, %[wk_lo]\n"
"s_mov_b32 s41, %[wk_hi]\n"
"s_mov_b32 s42, 4186368\n"
"ds_read_b64_tr_b16 a[48:49], v119\n"
"ds_read_b64_tr_b16 a[50:51], v119 offset:512\n"
"s_add_u32 m0, s66, 98304\n"
"s_add_u32 s52, s65, 2097152\n"
"buffer_load_dwordx4 v105, s[40:43], s52 offen lds\n"
"ds_read_b64_tr_b16 a[52:53], v119 offset:4096\n"
"ds_read_b64_tr_b16 a[54:55], v119 offset:4608\n"
"s_add_u32 m0, s66, 102400\n"
"s_add_u32 s52, s65, 2228224\n"
"buffer_load_dwordx4 v105, s[40:43], s52 offen lds\n"
"ds_read_b64_tr_b16 a[56:57], v119 offset:16384\n"
"ds_read_b64_tr_b16 a[58:59], v119 offset:16896\n"
"s_add_u32 m0, s66, 106496\n"
"s_add_u32 s52, s65, 2359296\n"
"buffer_load_dwordx4 v106, s[40:43], s52 offen lds\n"
"ds_read_b64_tr_b16 a[60:61], v119 offset:20480\n"
"ds_read_b64_tr_b16 a[62:63], v119 offset:20992\n"
"s_add_u32 m0, s66, 110592\n"
"s_add_u32 s52, s65, 2490368\n"
"buffer_load_dwordx4 v106, s[40:43], s52 offen lds\n"
"ds_read_b64_tr_b16 a[112:113], v120\n"
"ds_read_b64_tr_b16 a[114:115], v120 offset:512\n"
"s_add_u32 m0, s66, 114688\n"
"s_add_u32 s52, s65, 2621440\n"
"buffer_load_dwordx4 v105, s[40:43], s52 offen lds\n"
"ds_read_b64_tr_b16 a[116:117], v120 offset:4096\n"
"ds_read_b64_tr_b16 a[118:119], v120 offset:4608\n"
"s_add_u32 m0, s66, 118784\n"
"s_add_u32 s52, s65, 2752512\n"
"buffer_load_dwordx4 v105, s[40:43], s52 offen lds\n"
"ds_read_b64_tr_b16 a[120:121], v120 offset:16384\n"
"ds_read_b64_tr_b16 a[122:123], v120 offset:16896\n"
"s_add_u32 m0, s66, 122880\n"
"s_add_u32 s52, s65, 2883584\n"
"buffer_load_dwordx4 v106, s[40:43], s52 offen lds\n"
"s_waitcnt lgkmcnt(8)\n"
"ds_read_b64_tr_b16 a[124:125], v120 offset:20480\n"
"ds_read_b64_tr_b16 a[126:127], v120 offset:20992\n"
"s_add_u32 m0, s66, 126976\n"
"s_add_u32 s52, s65, 3014656\n"
"buffer_load_dwordx4 v106, s[40:43], s52 offen lds\n"
"s_waitcnt lgkmcnt(0)\n"
"s_waitcnt vmcnt(16)\n"
"s_barrier\n"
"v_add_u32 v119, 32768, v109\n"
"v_add_u32 v120, 32768, v110\n"
"s_mov_b32 s40, %[wk_lo]\n"
"s_mov_b32 s41, %[wk_hi]\n"
"s_mov_b32 s42, 4186368\n"
"ds_read_b64_tr_b16 a[128:129], v119\n"
"ds_read_b64_tr_b16 a[130:131], v119 offset:512\n"
"s_add_u32 m0, s66, 131072\n"
"s_add_u32 s52, s65, 3145728\n"
"buffer_load_dwordx4 v105, s[40:43], s52 offen lds\n"
"ds_read_b64_tr_b16 a[132:133], v119 offset:4096\n"
"ds_read_b64_tr_b16 a[134:135], v119 offset:4608\n"
"s_add_u32 m0, s66, 135168\n"
"s_add_u32 s52, s65, 3276800\n"
"buffer_load_dwordx4 v105, s[40:43], s52 offen lds\n"
"ds_read_b64_tr_b16 a[136:137], v119 offset:16384\n"
"ds_read_b64_tr_b16 a[138:139], v119 offset:16896\n"
"s_add_u32 m0, s66, 139264\n"
"s_add_u32 s52, s65, 3407872\n"
"buffer_load_dwordx4 v106, s[40:43], s52 offen lds\n"
"ds_read_b64_tr_b16 a[140:141], v119 offset:20480\n"
"ds_read_b64_tr_b16 a[142:143], v119 offset:20992\n"
"s_add_u32 m0, s66, 143360\n"
"s_add_u32 s52, s65, 3538944\n"
"buffer_load_dwordx4 v106, s[40:43], s52 offen lds\n"
"ds_read_b64_tr_b16 a[192:193], v120\n"
"ds_read_b64_tr_b16 a[194:195], v120 offset:512\n"
"s_add_u32 m0, s66, 147456\n"
"s_add_u32 s52, s65, 3670016\n"
"buffer_load_dwordx4 v105, s[40:43], s52 offen lds\n"
"ds_read_b64_tr_b16 a[196:197], v120 offset:4096\n"
"ds_read_b64_tr_b16 a[198:199], v120 offset:4608\n"
"s_add_u32 m0, s66, 151552\n"
"s_add_u32 s52, s65, 3801088\n"
"buffer_load_dwordx4 v105, s[40:43], s52 offen lds\n"
"ds_read_b64_tr_b16 a[200:201], v120 offset:16384\n"
"ds_read_b64_tr_b16 a[202:203], v120 offset:16896\n"
"s_add_u32 m0, s66, 155648\n"
"s_add_u32 s52, s65, 3932160\n"
"buffer_load_dwordx4 v106, s[40:43], s52 offen lds\n"
"s_waitcnt lgkmcnt(8)\n"
"ds_read_b64_tr_b16 a[204:205], v120 offset:20480\n"
"ds_read_b64_tr_b16 a[206:207], v120 offset:20992\n"
"s_add_u32 m0, s66, 159744\n"
"s_add_u32 s52, s65, 4063232\n"
"buffer_load_dwordx4 v106, s[40:43], s52 offen lds\n"
"s_waitcnt lgkmcnt(0)\n"
"s_waitcnt vmcnt(16)\n"
"s_barrier\n"
"v_add_u32 v119, 65536, v109\n"
"v_add_u32 v120, 65536, v110\n"
"s_mov_b32 s40, %[wv_lo]\n"
"s_mov_b32 s41, %[wv_hi]\n"
"s_mov_b32 s42, 4186368\n"
"ds_read_b64_tr_b16 a[144:145], v119\n"
"ds_read_b64_tr_b16 a[146:147], v119 offset:512\n"
"s_add_u32 m0, s66, 32768\n"
"s_add_u32 s52, s65, 0\n"
"buffer_load_dwordx4 v105, s[40:43], s52 offen lds\n"
"ds_read_b64_tr_b16 a[148:149], v119 offset:4096\n"
"ds_read_b64_tr_b16 a[150:151], v119 offset:4608\n"
"s_add_u32 m0, s66, 36864\n"
"s_add_u32 s52, s65, 131072\n"
"buffer_load_dwordx4 v105, s[40:43], s52 offen lds\n"
"ds_read_b64_tr_b16 a[152:153], v119 offset:16384\n"
"ds_read_b64_tr_b16 a[154:155], v119 offset:16896\n"
"s_add_u32 m0, s66, 40960\n"
"s_add_u32 s52, s65, 262144\n"
"buffer_load_dwordx4 v106, s[40:43], s52 offen lds\n"
"ds_read_b64_tr_b16 a[156:157], v119 offset:20480\n"
"ds_read_b64_tr_b16 a[158:159], v119 offset:20992\n"
"s_add_u32 m0, s66, 45056\n"
"s_add_u32 s52, s65, 393216\n"
"buffer_load_dwordx4 v106, s[40:43], s52 offen lds\n"
"ds_read_b64_tr_b16 a[208:209], v120\n"
"ds_read_b64_tr_b16 a[210:211], v120 offset:512\n"
"s_add_u32 m0, s66, 49152\n"
"s_add_u32 s52, s65, 524288\n"
"buffer_load_dwordx4 v105, s[40:43], s52 offen lds\n"
"ds_read_b64_tr_b16 a[212:213], v120 offset:4096\n"
"ds_read_b64_tr_b16 a[214:215], v120 offset:4608\n"
"s_add_u32 m0, s66, 53248\n"
"s_add_u32 s52, s65, 655360\n"
"buffer_load_dwordx4 v105, s[40:43], s52 offen lds\n"
"ds_read_b64_tr_b16 a[216:217], v120 offset:16384\n"
"ds_read_b64_tr_b16 a[218:219], v120 offset:16896\n"
"s_add_u32 m0, s66, 57344\n"
"s_add_u32 s52, s65, 786432\n"
"buffer_load_dwordx4 v106, s[40:43], s52 offen lds\n"
"s_waitcnt lgkmcnt(8)\n"
"ds_read_b64_tr_b16 a[220:221], v120 offset:20480\n"
"ds_read_b64_tr_b16 a[222:223], v120 offset:20992\n"
"s_add_u32 m0, s66, 61440\n"
"s_add_u32 s52, s65, 917504\n"
"buffer_load_dwordx4 v106, s[40:43], s52 offen lds\n"
"s_waitcnt lgkmcnt(0)\n"
"s_waitcnt vmcnt(16)\n"
"s_barrier\n"
"v_add_u32 v119, 98304, v109\n"
"v_add_u32 v120, 98304, v110\n"
"s_mov_b32 s40, %[wv_lo]\n"
"s_mov_b32 s41, %[wv_hi]\n"
"s_mov_b32 s42, 4186368\n"
"ds_read_b64_tr_b16 a[160:161], v119\n"
"ds_read_b64_tr_b16 a[162:163], v119 offset:512\n"
"s_add_u32 m0, s66, 65536\n"
"s_add_u32 s52, s65, 1048576\n"
"buffer_load_dwordx4 v105, s[40:43], s52 offen lds\n"
"ds_read_b64_tr_b16 a[164:165], v119 offset:4096\n"
"ds_read_b64_tr_b16 a[166:167], v119 offset:4608\n"
"s_add_u32 m0, s66, 69632\n"
"s_add_u32 s52, s65, 1179648\n"
"buffer_load_dwordx4 v105, s[40:43], s52 offen lds\n"
"ds_read_b64_tr_b16 a[168:169], v119 offset:16384\n"
"ds_read_b64_tr_b16 a[170:171], v119 offset:16896\n"
"s_add_u32 m0, s66, 73728\n"
"s_add_u32 s52, s65, 1310720\n"
"buffer_load_dwordx4 v106, s[40:43], s52 offen lds\n"
"ds_read_b64_tr_b16 a[172:173], v119 offset:20480\n"
"ds_read_b64_tr_b16 a[174:175], v119 offset:20992\n"
"s_add_u32 m0, s66, 77824\n"
"s_add_u32 s52, s65, 1441792\n"
"buffer_load_dwordx4 v106, s[40:43], s52 offen lds\n"
"ds_read_b64_tr_b16 a[224:225], v120\n"
"ds_read_b64_tr_b16 a[226:227], v120 offset:512\n"
"s_add_u32 m0, s66, 81920\n"
"s_add_u32 s52, s65, 1572864\n"
"buffer_load_dwordx4 v105, s[40:43], s52 offen lds\n"
"ds_read_b64_tr_b16 a[228:229], v120 offset:4096\n"
"ds_read_b64_tr_b16 a[230:231], v120 offset:4608\n"
"s_add_u32 m0, s66, 86016\n"
"s_add_u32 s52, s65, 1703936\n"
"buffer_load_dwordx4 v105, s[40:43], s52 offen lds\n"
"ds_read_b64_tr_b16 a[232:233], v120 offset:16384\n"
"ds_read_b64_tr_b16 a[234:235], v120 offset:16896\n"
"s_add_u32 m0, s66, 90112\n"
"s_add_u32 s52, s65, 1835008\n"
"buffer_load_dwordx4 v106, s[40:43], s52 offen lds\n"
"s_waitcnt lgkmcnt(8)\n"
"ds_read_b64_tr_b16 a[236:237], v120 offset:20480\n"
"ds_read_b64_tr_b16 a[238:239], v120 offset:20992\n"
"s_add_u32 m0, s66, 94208\n"
"s_add_u32 s52, s65, 1966080\n"
"buffer_load_dwordx4 v106, s[40:43], s52 offen lds\n"
"s_waitcnt lgkmcnt(0)\n"
"s_waitcnt vmcnt(16)\n"
"s_barrier\n"
"v_add_u32 v119, 131072, v109\n"
"v_add_u32 v120, 131072, v110\n"
"s_mov_b32 s40, %[wv_lo]\n"
"s_mov_b32 s41, %[wv_hi]\n"
"s_mov_b32 s42, 4186368\n"
"ds_read_b64_tr_b16 a[176:177], v119\n"
"ds_read_b64_tr_b16 a[178:179], v119 offset:512\n"
"s_add_u32 m0, s66, 98304\n"
"s_add_u32 s52, s65, 2097152\n"
"buffer_load_dwordx4 v105, s[40:43], s52 offen lds\n"
"ds_read_b64_tr_b16 a[180:181], v119 offset:4096\n"
"ds_read_b64_tr_b16 a[182:183], v119 offset:4608\n"
"s_add_u32 m0, s66, 102400\n"
"s_add_u32 s52, s65, 2228224\n"
"buffer_load_dwordx4 v105, s[40:43], s52 offen lds\n"
"ds_read_b64_tr_b16 a[184:185], v119 offset:16384\n"
"ds_read_b64_tr_b16 a[186:187], v119 offset:16896\n"
"s_add_u32 m0, s66, 106496\n"
"s_add_u32 s52, s65, 2359296\n"
"buffer_load_dwordx4 v106, s[40:43], s52 offen lds\n"
"ds_read_b64_tr_b16 a[188:189], v119 offset:20480\n"
"ds_read_b64_tr_b16 a[190:191], v119 offset:20992\n"
"s_add_u32 m0, s66, 110592\n"
"s_add_u32 s52, s65, 2490368\n"
"buffer_load_dwordx4 v106, s[40:43], s52 offen lds\n"
"ds_read_b64_tr_b16 a[240:241], v120\n"
"ds_read_b64_tr_b16 a[242:243], v120 offset:512\n"
"s_add_u32 m0, s66, 114688\n"
"s_add_u32 s52, s65, 2621440\n"
"buffer_load_dwordx4 v105, s[40:43], s52 offen lds\n"
"ds_read_b64_tr_b16 a[244:245], v120 offset:4096\n"
"ds_read_b64_tr_b16 a[246:247], v120 offset:4608\n"
"s_add_u32 m0, s66, 118784\n"
"s_add_u32 s52, s65, 2752512\n"
"buffer_load_dwordx4 v105, s[40:43], s52 offen lds\n"
"ds_read_b64_tr_b16 a[248:249], v120 offset:16384\n"
"ds_read_b64_tr_b16 a[250:251], v120 offset:16896\n"
"s_add_u32 m0, s66, 122880\n"
"s_add_u32 s52, s65, 2883584\n"
"buffer_load_dwordx4 v106, s[40:43], s52 offen lds\n"
"s_waitcnt lgkmcnt(8)\n"
"ds_read_b64_tr_b16 a[252:253], v120 offset:20480\n"
"ds_read_b64_tr_b16 a[254:255], v120 offset:20992\n"
"s_add_u32 m0, s66, 126976\n"
"s_add_u32 s52, s65, 3014656\n"
"buffer_load_dwordx4 v106, s[40:43], s52 offen lds\n"
"s_waitcnt lgkmcnt(0)\n"
"s_waitcnt vmcnt(16)\n"
"s_barrier\n"
"v_add_u32 v119, 32768, v109\n"
"v_add_u32 v120, 32768, v110\n"
"s_mov_b32 s40, %[wv_lo]\n"
"s_mov_b32 s41, %[wv_hi]\n"
"s_mov_b32 s42, 4186368\n"
"ds_read_b64_tr_b16 v[128:129], v119\n"
"ds_read_b64_tr_b16 v[130:131], v119 offset:512\n"
"s_add_u32 m0, s66, 131072\n"
"s_add_u32 s52, s65, 3145728\n"
"buffer_load_dwordx4 v105, s[40:43], s52 offen lds\n"
"ds_read_b64_tr_b16 v[132:133], v119 offset:4096\n"
"ds_read_b64_tr_b16 v[134:135], v119 offset:4608\n"
"s_add_u32 m0, s66, 135168\n"
"s_add_u32 s52, s65, 3276800\n"
"buffer_load_dwordx4 v105, s[40:43], s52 offen lds\n"
"ds_read_b64_tr_b16 v[136:137], v119 offset:16384\n"
"ds_read_b64_tr_b16 v[138:139], v119 offset:16896\n"
"s_add_u32 m0, s66, 139264\n"
"s_add_u32 s52, s65, 3407872\n"
"buffer_load_dwordx4 v106, s[40:43], s52 offen lds\n"
"ds_read_b64_tr_b16 v[140:141], v119 offset:20480\n"
"ds_read_b64_tr_b16 v[142:143], v119 offset:20992\n"
"s_add_u32 m0, s66, 143360\n"
"s_add_u32 s52, s65, 3538944\n"
"buffer_load_dwordx4 v106, s[40:43], s52 offen lds\n"
"ds_read_b64_tr_b16 v[192:193], v120\n"
"ds_read_b64_tr_b16 v[194:195], v120 offset:512\n"
"s_add_u32 m0, s66, 147456\n"
"s_add_u32 s52, s65, 3670016\n"
"buffer_load_dwordx4 v105, s[40:43], s52 offen lds\n"
"ds_read_b64_tr_b16 v[196:197], v120 offset:4096\n"
"ds_read_b64_tr_b16 v[198:199], v120 offset:4608\n"
"s_add_u32 m0, s66, 151552\n"
"s_add_u32 s52, s65, 3801088\n"
"buffer_load_dwordx4 v105, s[40:43], s52 offen lds\n"
"ds_read_b64_tr_b16 v[200:201], v120 offset:16384\n"
"ds_read_b64_tr_b16 v[202:203], v120 offset:16896\n"
"s_add_u32 m0, s66, 155648\n"
"s_add_u32 s52, s65, 3932160\n"
"buffer_load_dwordx4 v106, s[40:43], s52 offen lds\n"
"s_waitcnt lgkmcnt(8)\n"
"ds_read_b64_tr_b16 v[204:205], v120 offset:20480\n"
"ds_read_b64_tr_b16 v[206:207], v120 offset:20992\n"
"s_add_u32 m0, s66, 159744\n"
"s_add_u32 s52, s65, 4063232\n"
"buffer_load_dwordx4 v106, s[40:43], s52 offen lds\n"
"s_waitcnt lgkmcnt(0)\n"
"s_waitcnt vmcnt(16)\n"
"s_barrier\n"
"v_add_u32 v119, 65536, v109\n"
"v_add_u32 v120, 65536, v110\n"
"s_mov_b32 s40, %[wo_lo]\n"
"s_mov_b32 s41, %[wo_hi]\n"
"s_mov_b32 s42, 130560\n"
"ds_read_b64_tr_b16 v[144:145], v119\n"
"ds_read_b64_tr_b16 v[146:147], v119 offset:512\n"
"s_add_u32 m0, s66, 32768\n"
"s_add_u32 s52, s67, 0\n"
"buffer_load_dwordx4 v107, s[40:43], s52 offen lds\n"
"ds_read_b64_tr_b16 v[148:149], v119 offset:4096\n"
"ds_read_b64_tr_b16 v[150:151], v119 offset:4608\n"
"s_add_u32 m0, s66, 36864\n"
"s_add_u32 s52, s67, 8192\n"
"buffer_load_dwordx4 v108, s[40:43], s52 offen lds\n"
"ds_read_b64_tr_b16 v[152:153], v119 offset:16384\n"
"ds_read_b64_tr_b16 v[154:155], v119 offset:16896\n"
"s_add_u32 m0, s66, 40960\n"
"s_add_u32 s52, s67, 16384\n"
"buffer_load_dwordx4 v107, s[40:43], s52 offen lds\n"
"ds_read_b64_tr_b16 v[156:157], v119 offset:20480\n"
"ds_read_b64_tr_b16 v[158:159], v119 offset:20992\n"
"s_add_u32 m0, s66, 45056\n"
"s_add_u32 s52, s67, 24576\n"
"buffer_load_dwordx4 v108, s[40:43], s52 offen lds\n"
"ds_read_b64_tr_b16 v[208:209], v120\n"
"ds_read_b64_tr_b16 v[210:211], v120 offset:512\n"
"s_add_u32 m0, s66, 49152\n"
"s_add_u32 s52, s67, 32768\n"
"buffer_load_dwordx4 v107, s[40:43], s52 offen lds\n"
"ds_read_b64_tr_b16 v[212:213], v120 offset:4096\n"
"ds_read_b64_tr_b16 v[214:215], v120 offset:4608\n"
"s_add_u32 m0, s66, 53248\n"
"s_add_u32 s52, s67, 40960\n"
"buffer_load_dwordx4 v108, s[40:43], s52 offen lds\n"
"ds_read_b64_tr_b16 v[216:217], v120 offset:16384\n"
"ds_read_b64_tr_b16 v[218:219], v120 offset:16896\n"
"s_add_u32 m0, s66, 57344\n"
"s_add_u32 s52, s67, 49152\n"
"buffer_load_dwordx4 v107, s[40:43], s52 offen lds\n"
"s_waitcnt lgkmcnt(8)\n"
"ds_read_b64_tr_b16 v[220:221], v120 offset:20480\n"
"ds_read_b64_tr_b16 v[222:223], v120 offset:20992\n"
"s_add_u32 m0, s66, 61440\n"
"s_add_u32 s52, s67, 57344\n"
"buffer_load_dwordx4 v108, s[40:43], s52 offen lds\n"
"s_waitcnt lgkmcnt(0)\n"
"s_waitcnt vmcnt(16)\n"
"s_barrier\n"
"v_add_u32 v119, 98304, v109\n"
"v_add_u32 v120, 98304, v110\n"
"s_mov_b32 s40, %[wo_lo]\n"
"s_mov_b32 s41, %[wo_hi]\n"
"s_mov_b32 s42, 130560\n"
"ds_read_b64_tr_b16 v[160:161], v119\n"
"ds_read_b64_tr_b16 v[162:163], v119 offset:512\n"
"s_add_u32 m0, s66, 65536\n"
"s_add_u32 s52, s67, 65536\n"
"buffer_load_dwordx4 v107, s[40:43], s52 offen lds\n"
"ds_read_b64_tr_b16 v[164:165], v119 offset:4096\n"
"ds_read_b64_tr_b16 v[166:167], v119 offset:4608\n"
"s_add_u32 m0, s66, 69632\n"
"s_add_u32 s52, s67, 73728\n"
"buffer_load_dwordx4 v108, s[40:43], s52 offen lds\n"
"ds_read_b64_tr_b16 v[168:169], v119 offset:16384\n"
"ds_read_b64_tr_b16 v[170:171], v119 offset:16896\n"
"s_add_u32 m0, s66, 73728\n"
"s_add_u32 s52, s67, 81920\n"
"buffer_load_dwordx4 v107, s[40:43], s52 offen lds\n"
"ds_read_b64_tr_b16 v[172:173], v119 offset:20480\n"
"ds_read_b64_tr_b16 v[174:175], v119 offset:20992\n"
"s_add_u32 m0, s66, 77824\n"
"s_add_u32 s52, s67, 90112\n"
"buffer_load_dwordx4 v108, s[40:43], s52 offen lds\n"
"ds_read_b64_tr_b16 v[224:225], v120\n"
"ds_read_b64_tr_b16 v[226:227], v120 offset:512\n"
"s_add_u32 m0, s66, 81920\n"
"s_add_u32 s52, s67, 98304\n"
"buffer_load_dwordx4 v107, s[40:43], s52 offen lds\n"
"ds_read_b64_tr_b16 v[228:229], v120 offset:4096\n"
"ds_read_b64_tr_b16 v[230:231], v120 offset:4608\n"
"s_add_u32 m0, s66, 86016\n"
"s_add_u32 s52, s67, 106496\n"
"buffer_load_dwordx4 v108, s[40:43], s52 offen lds\n"
"ds_read_b64_tr_b16 v[232:233], v120 offset:16384\n"
"ds_read_b64_tr_b16 v[234:235], v120 offset:16896\n"
"s_add_u32 m0, s66, 90112\n"
"s_add_u32 s52, s67, 114688\n"
"buffer_load_dwordx4 v107, s[40:43], s52 offen lds\n"
"s_waitcnt lgkmcnt(8)\n"
"ds_read_b64_tr_b16 v[236:237], v120 offset:20480\n"
"ds_read_b64_tr_b16 v[238:239], v120 offset:20992\n"
"s_add_u32 m0, s66, 94208\n"
"s_add_u32 s52, s67, 122880\n"
"buffer_load_dwordx4 v108, s[40:43], s52 offen lds\n"
"s_waitcnt lgkmcnt(0)\n"
"s_waitcnt vmcnt(16)\n"
"s_barrier\n"
"v_add_u32 v119, 131072, v109\n"
"v_add_u32 v120, 131072, v110\n"
"ds_read_b64_tr_b16 v[176:177], v119\n"
"ds_read_b64_tr_b16 v[178:179], v119 offset:512\n"
"ds_read_b64_tr_b16 v[180:181], v119 offset:4096\n"
"ds_read_b64_tr_b16 v[182:183], v119 offset:4608\n"
"ds_read_b64_tr_b16 v[184:185], v119 offset:16384\n"
"ds_read_b64_tr_b16 v[186:187], v119 offset:16896\n"
"ds_read_b64_tr_b16 v[188:189], v119 offset:20480\n"
"ds_read_b64_tr_b16 v[190:191], v119 offset:20992\n"
"ds_read_b64_tr_b16 v[240:241], v120\n"
"ds_read_b64_tr_b16 v[242:243], v120 offset:512\n"
"ds_read_b64_tr_b16 v[244:245], v120 offset:4096\n"
"ds_read_b64_tr_b16 v[246:247], v120 offset:4608\n"
"ds_read_b64_tr_b16 v[248:249], v120 offset:16384\n"
"ds_read_b64_tr_b16 v[250:251], v120 offset:16896\n"
"s_waitcnt lgkmcnt(8)\n"
"ds_read_b64_tr_b16 v[252:253], v120 offset:20480\n"
"ds_read_b64_tr_b16 v[254:255], v120 offset:20992\n"
"s_waitcnt lgkmcnt(0)\n"
"s_waitcnt vmcnt(8)\n"
"s_barrier\n"
"s_lshl_b32 s52, %[w], 14\n"
"s_add_u32 s52, s52, 98304\n"
"v_lshlrev_b32 v119, 4, v100\n"
"v_add_u32 v119, s52, v119\n"
"v_add_u32 v120, 0, v111\n"
"ds_read_b64_tr_b16 v[28:29], v120\n"
"ds_read_b64_tr_b16 v[30:31], v120 offset:1024\n"
"ds_read_b64_tr_b16 v[32:33], v120 offset:16384\n"
"ds_read_b64_tr_b16 v[34:35], v120 offset:17408\n"
"v_add_u32 v120, 0, v112\n"
"ds_read_b64_tr_b16 v[36:37], v120\n"
"ds_read_b64_tr_b16 v[38:39], v120 offset:1024\n"
"ds_read_b64_tr_b16 v[40:41], v120 offset:16384\n"
"ds_read_b64_tr_b16 v[42:43], v120 offset:17408\n"
"v_add_u32 v120, 0, v113\n"
"ds_read_b64_tr_b16 v[44:45], v120\n"
"ds_read_b64_tr_b16 v[46:47], v120 offset:1024\n"
"ds_read_b64_tr_b16 v[48:49], v120 offset:16384\n"
"ds_read_b64_tr_b16 v[50:51], v120 offset:17408\n"
"v_add_u32 v120, 0, v114\n"
"ds_read_b64_tr_b16 v[52:53], v120\n"
"ds_read_b64_tr_b16 v[54:55], v120 offset:1024\n"
"s_waitcnt lgkmcnt(8)\n"
"ds_read_b64_tr_b16 v[56:57], v120 offset:16384\n"
"ds_read_b64_tr_b16 v[58:59], v120 offset:17408\n"
"s_waitcnt lgkmcnt(0)\n"
"ds_write_b128 v119, v[28:31]\n"
"ds_write_b128 v119, v[32:35] offset:1024\n"
"ds_write_b128 v119, v[36:39] offset:4096\n"
"ds_write_b128 v119, v[40:43] offset:5120\n"
"ds_write_b128 v119, v[44:47] offset:8192\n"
"ds_write_b128 v119, v[48:51] offset:9216\n"
"ds_write_b128 v119, v[52:55] offset:12288\n"
"ds_write_b128 v119, v[56:59] offset:13312\n"
"s_waitcnt lgkmcnt(0)\n"
"s_waitcnt vmcnt(0)\n"
"s_barrier\n"
"s_lshl_b32 s52, %[w], 14\n"
"s_add_u32 s52, s52, 98304\n"
"v_lshlrev_b32 v119, 4, v100\n"
"v_add_u32 v119, s52, v119\n"
"v_add_u32 v120, 32768, v111\n"
"ds_read_b64_tr_b16 v[28:29], v120\n"
"ds_read_b64_tr_b16 v[30:31], v120 offset:1024\n"
"ds_read_b64_tr_b16 v[32:33], v120 offset:16384\n"
"ds_read_b64_tr_b16 v[34:35], v120 offset:17408\n"
"v_add_u32 v120, 32768, v112\n"
"ds_read_b64_tr_b16 v[36:37], v120\n"
"ds_read_b64_tr_b16 v[38:39], v120 offset:1024\n"
"ds_read_b64_tr_b16 v[40:41], v120 offset:16384\n"
"ds_read_b64_tr_b16 v[42:43], v120 offset:17408\n"
"v_add_u32 v120, 32768, v113\n"
"ds_read_b64_tr_b16 v[44:45], v120\n"
"ds_read_b64_tr_b16 v[46:47], v120 offset:1024\n"
"ds_read_b64_tr_b16 v[48:49], v120 offset:16384\n"
"ds_read_b64_tr_b16 v[50:51], v120 offset:17408\n"
"v_add_u32 v120, 32768, v114\n"
"ds_read_b64_tr_b16 v[52:53], v120\n"
"ds_read_b64_tr_b16 v[54:55], v120 offset:1024\n"
"s_waitcnt lgkmcnt(8)\n"
"ds_read_b64_tr_b16 v[56:57], v120 offset:16384\n"
"ds_read_b64_tr_b16 v[58:59], v120 offset:17408\n"
"s_waitcnt lgkmcnt(0)\n"
"ds_write_b128 v119, v[28:31] offset:2048\n"
"ds_write_b128 v119, v[32:35] offset:3072\n"
"ds_write_b128 v119, v[36:39] offset:6144\n"
"ds_write_b128 v119, v[40:43] offset:7168\n"
"ds_write_b128 v119, v[44:47] offset:10240\n"
"ds_write_b128 v119, v[48:51] offset:11264\n"
"ds_write_b128 v119, v[52:55] offset:14336\n"
"ds_write_b128 v119, v[56:59] offset:15360\n"
"s_waitcnt lgkmcnt(0)\n"
"s_barrier\n"
"v_and_b32 v105, 15, v100\n"
"v_lshrrev_b32 v106, 4, v100\n"
"v_lshrrev_b32 v107, 5, v100\n"
"v_and_b32 v108, 1, v106\n"
"v_mov_b32 v101, 17476\n"
"v_mov_b32 v102, 61166\n"
"v_cmp_gt_u32 vcc, v105, 7\n"
"s_nop 1\n"
"v_cndmask_b32 v0, v101, v102, vcc\n"
"v_lshlrev_b32 v1, 4, v100\n"
"s_lshl_b32 s52, %[w], 11\n"
"v_add_u32 v2, s52, v1\n"
"s_lshl_b32 s52, %[w], 5\n"
"v_lshl_add_u32 v101, v107, 4, v105\n"
"v_add_u32 v101, s52, v101\n"
"v_lshlrev_b32 v101, 5, v101\n"
"v_add_u32 v101, 8192, v101\n"
"v_lshrrev_b32 v102, 2, v105\n"
"v_xor_b32 v103, v108, v102\n"
"v_lshl_add_u32 v4, v103, 3, v101\n"
"v_xor_b32 v3, 16, v4\n"
"v_bfe_u32 v101, v100, 2, 2\n"
"v_lshl_add_u32 v101, v106, 2, v101\n"
"v_lshlrev_b32 v101, 5, v101\n"
"v_and_b32 v102, 3, v100\n"
"v_xor_b32 v102, v102, v106\n"
"v_lshl_add_u32 v101, v102, 3, v101\n"
"v_add_u32 v5, 8192, v101\n"
"s_lshr_b32 s52, %[w], 1\n"
"s_lshl_b32 s52, s52, 10\n"
"s_and_b32 s53, %[w], 1\n"
"s_lshl_b32 s53, s53, 9\n"
"s_add_u32 s52, s52, s53\n"
"s_add_u32 s52, s52, 12288\n"
"v_lshrrev_b32 v101, 1, v106\n"
"v_lshlrev_b32 v101, 7, v101\n"
"v_and_b32 v102, 7, v100\n"
"v_lshl_add_u32 v101, v102, 4, v101\n"
"v_lshl_add_u32 v101, v108, 3, v101\n"
"v_add_u32 v6, s52, v101\n"
"s_lshl_b32 s52, %[w], 14\n"
"s_add_u32 s52, s52, 98304\n"
"v_add_u32 v7, s52, v1\n"
"s_lshl_b32 s52, %[g], 5\n"
"s_lshl_b32 s53, %[w], 3\n"
"s_add_u32 s52, s52, s53\n"
"v_lshrrev_b32 v101, 2, v105\n"
"v_lshl_add_u32 v101, v107, 2, v101\n"
"v_add_u32 v101, s52, v101\n"
"v_lshrrev_b32 v102, 1, v101\n"
"v_lshlrev_b32 v102, 14, v102\n"
"v_and_b32 v101, 1, v101\n"
"v_lshl_add_u32 v102, v101, 12, v102\n"
"v_and_b32 v101, 3, v105\n"
"v_lshl_add_u32 v101, v108, 2, v101\n"
"v_lshl_add_u32 v102, v101, 4, v102\n"
"s_lshl_b32 s52, %[h], 7\n"
"v_add_u32 v8, s52, v102\n"
"s_lshr_b32 s52, s60, 1\n"
"s_lshl_b32 s52, s52, 14\n"
"s_and_b32 s53, s60, 1\n"
"s_lshl_b32 s53, s53, 12\n"
"s_add_u32 s52, s52, s53\n"
"v_add_u32 v9, s52, v1\n"
"v_and_b32 v101, 3, v105\n"
"v_lshl_add_u32 v101, v108, 2, v101\n"
"v_lshlrev_b32 v101, 4, v101\n"
"v_bfe_u32 v102, v105, 2, 1\n"
"v_lshl_add_u32 v101, v102, 3, v101\n"
"v_lshl_add_u32 v101, v107, 2, v101\n"
"s_lshl_b32 s52, s60, 7\n"
"v_add_u32 v10, s52, v101\n"
"v_lshlrev_b32 v101, 7, v106\n"
"v_lshl_add_u32 v11, v105, 4, v101\n"
"s_lshl_b32 s52, %[w], 9\n"
"v_lshlrev_b32 v101, 3, v100\n"
"s_mul_i32 s53, %[g], 100663296\n"
"s_add_u32 s52, s52, s53\n"
"v_add_u32 v12, s52, v101\n"
"v_mov_b32 v101, 4286578688\n"
"v_mov_b32 v102, 0\n"
"v_cmp_lt_u32 vcc, 1, v106\n"
"s_nop 1\n"
"v_cndmask_b32 v16, v102, v101, vcc\n"
"v_mov_b32 v20, 1065369472\n"
"v_cndmask_b32 v17, v102, v101, vcc\n"
"v_mov_b32 v21, 1065369472\n"
"v_cndmask_b32 v18, v102, v101, vcc\n"
"v_mov_b32 v22, 1065369472\n"
"v_cndmask_b32 v19, v102, v101, vcc\n"
"v_mov_b32 v23, 1065369472\n"
"v_and_b32 v101, 1, v105\n"
"v_lshlrev_b32 v101, 4, v101\n"
"v_mov_b32 v103, 16256\n"
"v_lshlrev_b32 v103, v101, v103\n"
"v_lshrrev_b32 v101, 1, v105\n"
"v_cmp_eq_u32 vcc, 0, v101\n"
"s_nop 1\n"
"v_cndmask_b32 v24, v102, v103, vcc\n"
"v_cmp_eq_u32 vcc, 1, v101\n"
"s_nop 1\n"
"v_cndmask_b32 v25, v102, v103, vcc\n"
"v_cmp_eq_u32 vcc, 2, v101\n"
"s_nop 1\n"
"v_cndmask_b32 v26, v102, v103, vcc\n"
"v_cmp_eq_u32 vcc, 3, v101\n"
"s_nop 1\n"
"v_cndmask_b32 v27, v102, v103, vcc\n"
"s_mov_b32 s56, 0x3e0293ee\n"
"s_mov_b32 s57, 0xfffffffe\n"
"s_lshl_b32 s58, %[h], 7\n"
"s_add_u32 s58, s58, 2117632\n"
"s_mov_b32 s63, %[kprog]\n"
"s_mov_b32 s64, %[vprog]\n"
"s_mov_b32 s55, 400000\n"
"Lcv%=:\n"
"s_mov_b32 s52, 2098304\n"
"buffer_load_dword v127, off, s[36:39], s52 sc1\n"
"s_waitcnt vmcnt(0)\n"
"v_readfirstlane_b32 s53, v127\n"
"s_nop 3\n"
"s_cmp_le_u32 s53, 1\n"
"s_cbranch_scc1 Lcv_ok%=\n"
"s_sub_u32 s55, s55, 1\n"
"s_cmp_lg_u32 s55, 0\n"
"s_cbranch_scc1 Lcv%=\n"
"s_mov_b32 s52, 1\n"
"s_branch Lfail%=\n"
"Lcv_ok%=:\n"
"s_lshl_b32 s51, s53, 13\n"
"s_mov_b32 s44, 0\n"
"s_mov_b32 s45, s51\n"
"s_add_u32 s46, s51, 1048576\n"
"s_lshl_b32 s52, %[h], 13\n"
"s_add_u32 s47, s52, 4194304\n"
"s_add_u32 s48, s52, 20971520\n"
"s_lshl_b32 s52, %[h], 11\n"
"s_add_u32 s49, s52, 79691776\n"
"s_add_u32 s50, s52, 83886080\n"
"s_lshl_b32 s68, %[w], 11\n"
"v_mov_b32 v13, -1\n"
"v_mov_b32 v14, -1\n"
"s_mov_b32 s62, 0\n"
"v_min_i32 v15, v13, v14\n"
"s_mov_b32 s55, 400000\n"
"v_readfirstlane_b32 s52, v15\n"
"s_nop 3\n"
"Lpg241%=:\n"
"s_cmp_ge_i32 s52, s62\n"
"s_cbranch_scc1 Lpg_ok241%=\n"
"buffer_load_dword v13, off, s[36:39], s63 sc1\n"
"buffer_load_dword v14, off, s[36:39], s64 sc1\n"
"s_waitcnt vmcnt(0)\n"
"v_min_i32 v15, v13, v14\n"
"s_nop 1\n"
"v_readfirstlane_b32 s52, v15\n"
"s_nop 3\n"
"s_sub_u32 s55, s55, 1\n"
"s_cmp_lg_u32 s55, 0\n"
"s_cbranch_scc1 Lpg241%=\n"
"s_mov_b32 s52, 2\n"
"s_branch Lfail%=\n"
"Lpg_ok241%=:\n"
"s_add_u32 m0, s68, 16384\n"
"s_add_u32 s53, s47, 0\n"
"buffer_load_dwordx4 v2, s[36:39], s53 offen sc1 lds\n"
"s_add_u32 m0, s68, 17408\n"
"s_add_u32 s53, s47, 1024\n"
"buffer_load_dwordx4 v2, s[36:39], s53 offen sc1 lds\n"
"s_add_u32 s47, s47, 262144\n"
"s_add_u32 m0, s68, 24576\n"
"s_add_u32 s53, s48, 0\n"
"buffer_load_dwordx4 v2, s[36:39], s53 offen sc1 lds\n"
"s_add_u32 m0, s68, 25600\n"
"s_add_u32 s53, s48, 1024\n"
"buffer_load_dwordx4 v2, s[36:39], s53 offen sc1 lds\n"
"s_add_u32 s48, s48, 262144\n"
"buffer_load_dword v13, off, s[36:39], s63 sc1\n"
"buffer_load_dword v14, off, s[36:39], s64 sc1\n"
"s_mov_b32 s40, %[mem0_lo]\n"
"s_mov_b32 s41, %[mem0_hi]\n"
"s_mov_b32 s42, 8192\n"
"v_bfe_u32 v101, v100, 3, 2\n"
"v_lshlrev_b32 v101, 2, v101\n"
"v_lshl_add_u32 v101, v107, 4, v101\n"
"v_and_b32 v102, 7, v100\n"
"v_lshl_add_u32 v101, v102, 9, v101\n"
"v_lshlrev_b32 v101, 1, v101\n"
"s_lshl_b32 s52, %[w], 8\n"
"s_nop 0\n"
"buffer_load_dwordx2 v[92:93], v101, s[40:43], s52 offen\n"
"buffer_load_dwordx2 v[94:95], v101, s[40:43], s52 offen offset:64\n"
"buffer_load_dwordx2 v[96:97], v101, s[40:43], s52 offen offset:128\n"
"buffer_load_dwordx2 v[98:99], v101, s[40:43], s52 offen offset:192\n"
"v_mov_b32 v100, 0\n"
"v_mov_b32 v101, 0\n"
"v_mov_b32 v102, 0\n"
"v_mov_b32 v103, 0\n"
"v_mov_b32 v104, 0\n"
"v_mov_b32 v105, 0\n"
"v_mov_b32 v106, 0\n"
"v_mov_b32 v107, 0\n"
"v_mov_b32 v108, 0\n"
"v_mov_b32 v109, 0\n"
"v_mov_b32 v110, 0\n"
"v_mov_b32 v111, 0\n"
"v_mov_b32 v112, 0\n"
"v_mov_b32 v113, 0\n"
"v_mov_b32 v114, 0\n"
"v_mov_b32 v115, 0\n"
"v_mov_b32 v116, 0\n"
"v_mov_b32 v117, 0\n"
"v_mov_b32 v118, 0\n"
"v_mov_b32 v119, 0\n"
"v_mov_b32 v120, 0\n"
"v_mov_b32 v121, 0\n"
"v_mov_b32 v122, 0\n"
"v_mov_b32 v123, 0\n"
"s_waitcnt vmcnt(0)\n"
"s_branch LgotM%=\n"
"Lstep%=:\n"
"s_barrier\n"
"v_mov_b64 v[100:101], 0\n"
"v_mov_b64 v[102:103], 0\n"
"v_mov_b64 v[104:105], 0\n"
"v_mov_b64 v[106:107], 0\n"
"v_mov_b64 v[108:109], 0\n"
"v_mov_b64 v[110:111], 0\n"
"v_mov_b64 v[112:113], 0\n"
"v_mov_b64 v[114:115], 0\n"
"v_mov_b64 v[116:117], 0\n"
"v_mov_b64 v[118:119], 0\n"
"v_mov_b64 v[120:121], 0\n"
"v_mov_b64 v[122:123], 0\n"
"s_mov_b32 s59, %[d2]\n"
"s_cmp_eq_u32 s59, 0\n"
"s_cbranch_scc1 Ld_done242%=\n"
"Ld_loop242%=:\n"
"s_sub_u32 s59, s59, 1\n"
"s_cmp_lg_u32 s59, 0\n"
"s_cbranch_scc1 Ld_loop242%=\n"
"Ld_done242%=:\n"
"s_mov_b32 s55, 400000\n"
"LpollM%=:\n"
"buffer_load_dwordx4 v[92:95], v2, s[36:39], s45 offen sc1\n"
"buffer_load_dwordx4 v[96:99], v2, s[36:39], s45 offen offset:1024 sc1\n"
"s_waitcnt vmcnt(1)\n"
"v_max3_u32 v124, v92, v93, v94\n"
"v_max_u32 v124, v124, v95\n"
"s_waitcnt vmcnt(0)\n"
"v_max3_u32 v125, v96, v97, v98\n"
"v_max3_u32 v124, v124, v125, v99\n"
"v_cmp_eq_u32 vcc, -1, v124\n"
"s_cbranch_vccz LgotM%=\n"
"s_sub_u32 s55, s55, 1\n"
"s_cmp_lg_u32 s55, 0\n"
"s_cbranch_scc1 LpollM%=\n"
"s_mov_b32 s52, 3\n"
"s_branch Lfail%=\n"
"LgotM%=:\n"
"ds_write_b128 v2, v[92:95]\n"
"ds_write_b128 v2, v[96:99] offset:1024\n"
"s_waitcnt lgkmcnt(0)\n"
"s_barrier\n"
"ds_read_b128 v[28:31], v1\n"
"ds_read_b128 v[32:35], v1 offset:1024\n"
"ds_read_b128 v[36:39], v1 offset:2048\n"
"ds_read_b128 v[40:43], v1 offset:3072\n"
"ds_read_b128 v[44:47], v1 offset:4096\n"
"ds_read_b128 v[48:51], v1 offset:5120\n"
"ds_read_b128 v[52:55], v1 offset:6144\n"
"ds_read_b128 v[56:59], v1 offset:7168\n"
"s_waitcnt lgkmcnt(7)\n"
"v_smfmac_f32_16x16x64_bf16 v[100:103], v[28:31], a[0:7], v0\n"
"v_smfmac_f32_16x16x64_bf16 v[104:107], v[28:31], a[64:71], v0\n"
"v_smfmac_f32_16x16x64_bf16 v[108:111], v[28:31], a[128:135], v0\n"
"v_smfmac_f32_16x16x64_bf16 v[112:115], v[28:31], a[192:199], v0\n"
"s_waitcnt lgkmcnt(6)\n"
"v_smfmac_f32_16x16x64_bf16 v[100:103], v[32:35], a[8:15], v0\n"
"v_smfmac_f32_16x16x64_bf16 v[104:107], v[32:35], a[72:79], v0\n"
"v_smfmac_f32_16x16x64_bf16 v[108:111], v[32:35], a[136:143], v0\n"
"v_smfmac_f32_16x16x64_bf16 v[112:115], v[32:35], a[200:207], v0\n"
"s_waitcnt lgkmcnt(5)\n"
"v_smfmac_f32_16x16x64_bf16 v[100:103], v[36:39], a[16:23], v0\n"
"v_smfmac_f32_16x16x64_bf16 v[104:107], v[36:39], a[80:87], v0\n"
"v_smfmac_f32_16x16x64_bf16 v[108:111], v[36:39], a[144:151], v0\n"
"v_smfmac_f32_16x16x64_bf16 v[112:115], v[36:39], a[208:215], v0\n"
"ds_read_b128 v[60:63], v1 offset:16384\n"
"ds_read_b128 v[76:79], v1 offset:20480\n"
"s_waitcnt lgkmcnt(6)\n"
"v_smfmac_f32_16x16x64_bf16 v[100:103], v[40:43], a[24:31], v0\n"
"v_smfmac_f32_16x16x64_bf16 v[104:107], v[40:43], a[88:95], v0\n"
"v_smfmac_f32_16x16x64_bf16 v[108:111], v[40:43], a[152:159], v0\n"
"v_smfmac_f32_16x16x64_bf16 v[112:115], v[40:43], a[216:223], v0\n"
"ds_read_b128 v[64:67], v1 offset:17408\n"
"ds_read_b128 v[80:83], v1 offset:21504\n"
"s_waitcnt lgkmcnt(7)\n"
"v_smfmac_f32_16x16x64_bf16 v[100:103], v[44:47], a[32:39], v0\n"
"v_smfmac_f32_16x16x64_bf16 v[104:107], v[44:47], a[96:103], v0\n"
"v_smfmac_f32_16x16x64_bf16 v[108:111], v[44:47], a[160:167], v0\n"
"v_smfmac_f32_16x16x64_bf16 v[112:115], v[44:47], a[224:231], v0\n"
"ds_read_b128 v[68:71], v1 offset:18432\n"
"ds_read_b128 v[84:87], v1 offset:22528\n"
"s_waitcnt lgkmcnt(8)\n"
"v_smfmac_f32_16x16x64_bf16 v[100:103], v[48:51], a[40:47], v0\n"
"v_smfmac_f32_16x16x64_bf16 v[104:107], v[48:51], a[104:111], v0\n"
"v_smfmac_f32_16x16x64_bf16 v[108:111], v[48:51], a[168:175], v0\n"
"v_smfmac_f32_16x16x64_bf16 v[112:115], v[48:51], a[232:239], v0\n"
"ds_read_b128 v[72:75], v1 offset:19456\n"
"ds_read_b128 v[88:91], v1 offset:23552\n"
"s_waitcnt lgkmcnt(9)\n"
"v_smfmac_f32_16x16x64_bf16 v[100:103], v[52:55], a[48:55], v0\n"
"v_smfmac_f32_16x16x64_bf16 v[104:107], v[52:55], a[112:119], v0\n"
"v_smfmac_f32_16x16x64_bf16 v[108:111], v[52:55], a[176:183], v0\n"
"v_smfmac_f32_16x16x64_bf16 v[112:115], v[52:55], a[240:247], v0\n"
"ds_read_b128 v[92:95], v2 offset:24576\n"
"ds_read_b128 v[96:99], v2 offset:25600\n"
"s_waitcnt lgkmcnt(10)\n"
"v_smfmac_f32_16x16x64_bf16 v[100:103], v[56:59], a[56:63], v0\n"
"v_smfmac_f32_16x16x64_bf16 v[104:107], v[56:59], a[120:127], v0\n"
"v_smfmac_f32_16x16x64_bf16 v[108:111], v[56:59], a[184:191], v0\n"
"v_smfmac_f32_16x16x64_bf16 v[112:115], v[56:59], a[248:255], v0\n"
"v_smfmac_f32_16x16x64_bf16 v[116:119], v[28:31], v[128:135], v0\n"
"v_smfmac_f32_16x16x64_bf16 v[120:123], v[28:31], v[192:199], v0\n"
"v_smfmac_f32_16x16x64_bf16 v[116:119], v[32:35], v[136:143], v0\n"
"s_nop 2\n"
"v_permlane32_swap_b32 v100, v104\n"
"v_permlane32_swap_b32 v101, v105\n"
"v_permlane32_swap_b32 v102, v106\n"
"v_permlane32_swap_b32 v103, v107\n"
"v_permlane32_swap_b32 v108, v112\n"
"v_permlane32_swap_b32 v109, v113\n"
"v_permlane32_swap_b32 v110, v114\n"
"v_permlane32_swap_b32 v111, v115\n"
"v_pk_add_f32 v[100:101], v[100:101], v[104:105]\n"
"v_pk_add_f32 v[102:103], v[102:103], v[106:107]\n"
"v_pk_add_f32 v[108:109], v[108:109], v[112:113]\n"
"v_pk_add_f32 v[110:111], v[110:111], v[114:115]\n"
"v_cvt_pk_bf16_f32 v100, v100, v101\n"
"v_cvt_pk_bf16_f32 v101, v102, v103\n"
"v_cvt_pk_bf16_f32 v102, v108, v109\n"
"v_cvt_pk_bf16_f32 v103, v110, v111\n"
"ds_write_b64 v3, v[100:101]\n"
"ds_write_b64 v4, v[102:103]\n"
"v_smfmac_f32_16x16x64_bf16 v[120:123], v[32:35], v[200:207], v0\n"
"v_smfmac_f32_16x16x64_bf16 v[116:119], v[36:39], v[144:151], v0\n"
"v_smfmac_f32_16x16x64_bf16 v[120:123], v[36:39], v[208:215], v0\n"
"v_smfmac_f32_16x16x64_bf16 v[116:119], v[40:43], v[152:159], v0\n"
"v_smfmac_f32_16x16x64_bf16 v[120:123], v[40:43], v[216:223], v0\n"
"s_waitcnt lgkmcnt(0)\n"
"s_barrier\n"
"ds_read_b64_tr_b16 v[28:29], v5\n"
"ds_read_b64_tr_b16 v[30:31], v5 offset:512\n"
"ds_read_b64_tr_b16 v[32:33], v5 offset:1024\n"
"ds_read_b64_tr_b16 v[34:35], v5 offset:1536\n"
"ds_read_b64_tr_b16 v[36:37], v5 offset:2048\n"
"ds_read_b64_tr_b16 v[38:39], v5 offset:2560\n"
"ds_read_b64_tr_b16 v[40:41], v5 offset:3072\n"
"ds_read_b64_tr_b16 v[42:43], v5 offset:3584\n"
"v_smfmac_f32_16x16x64_bf16 v[116:119], v[44:47], v[160:167], v0\n"
"v_smfmac_f32_16x16x64_bf16 v[120:123], v[44:47], v[224:231], v0\n"
"v_smfmac_f32_16x16x64_bf16 v[116:119], v[48:51], v[168:175], v0\n"
"v_smfmac_f32_16x16x64_bf16 v[120:123], v[48:51], v[232:239], v0\n"
"v_smfmac_f32_16x16x64_bf16 v[116:119], v[52:55], v[176:183], v0\n"
"v_smfmac_f32_16x16x64_bf16 v[120:123], v[52:55], v[240:247], v0\n"
"ds_read_b128 v[44:47], v7 offset:4096\n"
"ds_read_b128 v[48:51], v7 offset:5120\n"
"ds_read_b128 v[52:55], v7 offset:6144\n"
"s_waitcnt lgkmcnt(9)\n"
"v_mfma_f32_16x16x32_bf16 v[100:103], v[60:63], v[28:31], 0\n"
"v_mfma_f32_16x16x32_bf16 v[104:107], v[76:79], v[28:31], 0\n"
"v_mfma_f32_16x16x32_bf16 v[108:111], v[28:31], v[28:31], v[16:19]\n"
"v_smfmac_f32_16x16x64_bf16 v[116:119], v[56:59], v[184:191], v0\n"
"ds_read_b128 v[28:31], v7\n"
"ds_read_b128 v[60:63], v7 offset:8192\n"
"ds_read_b128 v[76:79], v7 offset:12288\n"
"s_waitcnt lgkmcnt(10)\n"
"v_mfma_f32_16x16x32_bf16 v[100:103], v[64:67], v[32:35], v[100:103]\n"
"v_mfma_f32_16x16x32_bf16 v[104:107], v[80:83], v[32:35], v[104:107]\n"
"v_mfma_f32_16x16x32_bf16 v[108:111], v[32:35], v[32:35], v[108:111]\n"
"v_smfmac_f32_16x16x64_bf16 v[120:123], v[56:59], v[248:255], v0\n"
"ds_read_b128 v[32:35], v7 offset:1024\n"
"ds_read_b128 v[64:67], v7 offset:9216\n"
"ds_read_b128 v[80:83], v7 offset:13312\n"
"ds_read_b128 v[56:59], v7 offset:7168\n"
"s_waitcnt lgkmcnt(12)\n"
"v_mfma_f32_16x16x32_bf16 v[100:103], v[68:71], v[36:39], v[100:103]\n"
"v_mfma_f32_16x16x32_bf16 v[104:107], v[84:87], v[36:39], v[104:107]\n"
"v_mfma_f32_16x16x32_bf16 v[108:111], v[36:39], v[36:39], v[108:111]\n"
"ds_read_b128 v[36:39], v7 offset:2048\n"
"ds_read_b128 v[68:71], v7 offset:10240\n"
"s_waitcnt lgkmcnt(8)\n"
"ds_read_b128 v[84:87], v7 offset:14336\n"
"v_mfma_f32_16x16x32_bf16 v[100:103], v[72:75], v[40:43], v[100:103]\n"
"v_mfma_f32_16x16x32_bf16 v[104:107], v[88:91], v[40:43], v[104:107]\n"
"v_mfma_f32_16x16x32_bf16 v[108:111], v[40:43], v[40:43], v[108:111]\n"
"ds_read_b128 v[40:43], v7 offset:3072\n"
"ds_read_b128 v[72:75], v7 offset:11264\n"
"ds_read_b128 v[88:91], v7 offset:15360\n"
"s_cmp_eq_u32 s44, 63\n"
"v_permlane32_swap_b32 v116, v120\n"
"v_permlane32_swap_b32 v117, v121\n"
"v_permlane32_swap_b32 v118, v122\n"
"v_permlane32_swap_b32 v119, v123\n"
"v_pk_add_f32 v[116:117], v[116:117], v[120:121]\n"
"v_pk_add_f32 v[118:119], v[118:119], v[122:123]\n"
"v_cvt_pk_bf16_f32 v116, v116, v117\n"
"v_cvt_pk_bf16_f32 v117, v118, v119\n"
"buffer_store_dwordx2 v[116:117], v12, s[36:39], s50 offen sc1\n"
"s_cbranch_scc1 Llast%=\n"
"v_max3_f32 v124, v100, v101, v102\n"
"v_max3_f32 v125, v103, v104, v105\n"
"v_max3_f32 v126, v106, v107, v108\n"
"v_max3_f32 v127, v109, v110, v111\n"
"v_max3_f32 v124, v124, v125, v126\n"
"v_mov_b64 v[118:119], v[116:117]\n"
"v_max_f32 v124, v124, v127\n"
"v_mov_b32 v125, v124\n"
"v_permlane32_swap_b32 v116, v118\n"
"s_nop 0\n"
"v_permlane16_swap_b32 v124, v125\n"
"v_max_f32 v124, v124, v125\n"
"v_mov_b32 v125, v124\n"
"v_permlane32_swap_b32 v117, v119\n"
"s_nop 0\n"
"v_permlane32_swap_b32 v124, v125\n"
"v_max_f32 v124, v124, v125\n"
"v_mul_f32 v124, s56, v124\n"
"v_pk_fma_f32 v[100:101], v[100:101], s[56:57], v[124:125] op_sel_hi:[1,0,0] neg_lo:[0,0,1] neg_hi:[0,0,1]\n"
"v_pk_fma_f32 v[102:103], v[102:103], s[56:57], v[124:125] op_sel_hi:[1,0,0] neg_lo:[0,0,1] neg_hi:[0,0,1]\n"
"v_pk_fma_f32 v[104:105], v[104:105], s[56:57], v[124:125] op_sel_hi:[1,0,0] neg_lo:[0,0,1] neg_hi:[0,0,1]\n"
"v_pk_fma_f32 v[106:107], v[106:107], s[56:57], v[124:125] op_sel_hi:[1,0,0] neg_lo:[0,0,1] neg_hi:[0,0,1]\n"
"v_exp_f32 v100, v100\n"
"v_exp_f32 v101, v101\n"
"v_exp_f32 v102, v102\n"
"v_exp_f32 v103, v103\n"
"v_exp_f32 v104, v104\n"
"v_exp_f32 v105, v105\n"
"v_exp_f32 v106, v106\n"
"v_exp_f32 v107, v107\n"
"v_cvt_pk_bf16_f32 v120, v100, v101\n"
"v_cvt_pk_bf16_f32 v121, v102, v103\n"
"v_cvt_pk_bf16_f32 v122, v104, v105\n"
"v_cvt_pk_bf16_f32 v123, v106, v107\n"
"v_pk_fma_f32 v[108:109], v[108:109], s[56:57], v[124:125] op_sel_hi:[1,0,0] neg_lo:[0,0,1] neg_hi:[0,0,1]\n"
"v_pk_fma_f32 v[110:111], v[110:111], s[56:57], v[124:125] op_sel_hi:[1,0,0] neg_lo:[0,0,1] neg_hi:[0,0,1]\n"
"v_mfma_f32_16x16x32_bf16 v[124:127], v[20:23], v[120:123], 0\n"
"v_exp_f32 v108, v108\n"
"v_mfma_f32_16x16x32_bf16 v[100:103], v[92:95], v[120:123], 0\n"
"v_exp_f32 v109, v109\n"
"v_exp_f32 v110, v110\n"
"v_mfma_f32_16x16x32_bf16 v[104:107], v[96:99], v[120:123], 0\n"
"v_exp_f32 v111, v111\n"
"v_cvt_pk_bf16_f32 v112, v108, v109\n"
"v_cvt_pk_bf16_f32 v113, v110, v111\n"
"v_mov_b64 v[108:109], 0\n"
"s_nop 0\n"
"v_mfma_f32_16x16x16_bf16 v[124:127], v[20:21], v[112:113], v[124:127]\n"
"v_mfma_f32_16x16x16_bf16 v[100:103], v[116:117], v[112:113], v[100:103]\n"
"v_mov_b64 v[110:111], 0\n"
"v_mfma_f32_16x16x16_bf16 v[104:107], v[118:119], v[112:113], v[104:107]\n"
"v_mov_b64 v[114:115], 0\n"
"v_mov_b64 v[116:117], 0\n"
"v_mov_b64 v[118:119], 0\n"
"v_mov_b64 v[120:121], 0\n"
"v_mov_b64 v[122:123], 0\n"
"v_mov_b64 v[112:113], 0\n"
"v_rcp_f32 v124, v124\n"
"s_nop 0\n"
"v_pk_mul_f32 v[100:101], v[100:101], v[124:125] op_sel_hi:[1,0]\n"
"v_pk_mul_f32 v[102:103], v[102:103], v[124:125] op_sel_hi:[1,0]\n"
"v_pk_mul_f32 v[104:105], v[104:105], v[124:125] op_sel_hi:[1,0]\n"
"v_pk_mul_f32 v[106:107], v[106:107], v[124:125] op_sel_hi:[1,0]\n"
"v_cvt_pk_bf16_f32 v100, v100, v101\n"
"v_cvt_pk_bf16_f32 v101, v102, v103\n"
"s_mov_b32 exec_lo, 0xff00ff00\n"
"s_mov_b32 exec_hi, 0xff00ff00\n"
"v_cvt_pk_bf16_f32 v102, v104, v105\n"
"v_cvt_pk_bf16_f32 v103, v106, v107\n"
"ds_write_b64 v6, v[100:101]\n"
"ds_write_b64 v6, v[102:103] offset:256\n"
"s_mov_b64 exec, -1\n"
"s_add_u32 s62, s44, 1\n"
"s_cmp_ge_u32 s62, 63\n"
"s_cbranch_scc1 Lnok%=\n"
"v_min_i32 v15, v13, v14\n"
"s_mov_b32 s55, 400000\n"
"v_readfirstlane_b32 s52, v15\n"
"s_nop 3\n"
"Lpg291%=:\n"
"s_cmp_ge_i32 s52, s62\n"
"s_cbranch_scc1 Lpg_ok291%=\n"
"buffer_load_dword v13, off, s[36:39], s63 sc1\n"
"buffer_load_dword v14, off, s[36:39], s64 sc1\n"
"s_waitcnt vmcnt(0)\n"
"v_min_i32 v15, v13, v14\n"
"s_nop 1\n"
"v_readfirstlane_b32 s52, v15\n"
"s_nop 3\n"
"s_sub_u32 s55, s55, 1\n"
"s_cmp_lg_u32 s55, 0\n"
"s_cbranch_scc1 Lpg291%=\n"
"s_mov_b32 s52, 2\n"
"s_branch Lfail%=\n"
"Lpg_ok291%=:\n"
"s_add_u32 m0, s68, 16384\n"
"s_add_u32 s53, s47, 0\n"
"buffer_load_dwordx4 v2, s[36:39], s53 offen sc1 lds\n"
"s_add_u32 m0, s68, 17408\n"
"s_add_u32 s53, s47, 1024\n"
"buffer_load_dwordx4 v2, s[36:39], s53 offen sc1 lds\n"
"s_add_u32 s47, s47, 262144\n"
"Lnok%=:\n"
"s_waitcnt lgkmcnt(0)\n"
"s_barrier\n"
"ds_read_b128 v[100:103], v1 offset:12288\n"
"ds_read_b128 v[104:107], v1 offset:13312\n"
"s_cmp_ge_u32 s62, 63\n"
"s_cbranch_scc1 Lnov%=\n"
"s_add_u32 m0, s68, 24576\n"
"s_add_u32 s53, s48, 0\n"
"buffer_load_dwordx4 v2, s[36:39], s53 offen sc1 lds\n"
"s_add_u32 m0, s68, 25600\n"
"s_add_u32 s53, s48, 1024\n"
"buffer_load_dwordx4 v2, s[36:39], s53 offen sc1 lds\n"
"s_add_u32 s48, s48, 262144\n"
"Lnov%=:\n"
"s_waitcnt lgkmcnt(1)\n"
"v_smfmac_f32_16x16x64_bf16 v[108:111], v[100:103], v[28:35], v0\n"
"v_smfmac_f32_16x16x64_bf16 v[112:115], v[100:103], v[44:51], v0\n"
"s_waitcnt lgkmcnt(0)\n"
"v_smfmac_f32_16x16x64_bf16 v[108:111], v[104:107], v[36:43], v0\n"
"v_smfmac_f32_16x16x64_bf16 v[112:115], v[104:107], v[52:59], v0\n"
"v_smfmac_f32_16x16x64_bf16 v[116:119], v[100:103], v[60:67], v0\n"
"v_smfmac_f32_16x16x64_bf16 v[120:123], v[100:103], v[76:83], v0\n"
"v_smfmac_f32_16x16x64_bf16 v[116:119], v[104:107], v[68:75], v0\n"
"v_smfmac_f32_16x16x64_bf16 v[120:123], v[104:107], v[84:91], v0\n"
"s_nop 3\n"
"v_permlane32_swap_b32 v108, v112\n"
"v_permlane32_swap_b32 v109, v113\n"
"v_permlane32_swap_b32 v110, v114\n"
"v_permlane32_swap_b32 v111, v115\n"
"v_pk_add_f32 v[108:109], v[108:109], v[112:113]\n"
"v_pk_add_f32 v[110:111], v[110:111], v[114:115]\n"
"v_cvt_pk_bf16_f32 v124, v108, v109\n"
"v_cvt_pk_bf16_f32 v125, v110, v111\n"
"v_permlane32_swap_b32 v116, v120\n"
"v_permlane32_swap_b32 v117, v121\n"
"v_permlane32_swap_b32 v118, v122\n"
"v_permlane32_swap_b32 v119, v123\n"
"v_min_u32 v124, s57, v124\n"
"v_min_u32 v125, s57, v125\n"
"v_pk_add_f32 v[116:117], v[116:117], v[120:121]\n"
"v_pk_add_f32 v[118:119], v[118:119], v[122:123]\n"
"v_cvt_pk_bf16_f32 v126, v116, v117\n"
"v_cvt_pk_bf16_f32 v127, v118, v119\n"
"v_min_u32 v126, s57, v126\n"
"v_min_u32 v127, s57, v127\n"
"buffer_store_dwordx4 v[124:127], v8, s[36:39], s46 offen sc1\n"
"buffer_load_dword v13, off, s[36:39], s63 sc1\n"
"buffer_load_dword v14, off, s[36:39], s64 sc1\n"
"s_add_u32 s62, s45, 16384\n"
"s_cmp_eq_u32 %[w], 0\n"
"s_cbranch_scc0 Lside%=\n"
"v_mov_b64 v[104:105], -1\n"
"v_mov_b64 v[106:107], -1\n"
"s_xor_b32 s54, s46, 524288\n"
"buffer_store_dwordx4 v[104:107], v9, s[36:39], s54 offen sc1\n"
"buffer_store_dwordx4 v[104:107], v9, s[36:39], s54 offen offset:1024 sc1\n"
"buffer_store_dwordx4 v[104:107], v9, s[36:39], s54 offen offset:2048 sc1\n"
"buffer_store_dwordx4 v[104:107], v9, s[36:39], s54 offen offset:3072 sc1\n"
"s_mov_b32 s59, %[d1]\n"
"s_cmp_eq_u32 s59, 0\n"
"s_cbranch_scc1 Ld_done294%=\n"
"Ld_loop294%=:\n"
"s_sub_u32 s59, s59, 1\n"
"s_cmp_lg_u32 s59, 0\n"
"s_cbranch_scc1 Ld_loop294%=\n"
"Ld_done294%=:\n"
"s_mov_b32 s55, 400000\n"
"LpollP%=:\n"
"buffer_load_dwordx4 v[28:31], v9, s[36:39], s46 offen sc1\n"
"buffer_load_dwordx4 v[32:35], v9, s[36:39], s46 offen offset:1024 sc1\n"
"buffer_load_dwordx4 v[36:39], v9, s[36:39], s46 offen offset:2048 sc1\n"
"buffer_load_dwordx4 v[40:43], v9, s[36:39], s46 offen offset:3072 sc1\n"
"s_waitcnt vmcnt(3)\n"
"v_max3_u32 v104, v28, v29, v30\n"
"v_mfma_f32_16x16x32_bf16 v[100:103], v[28:31], v[24:27], 0\n"
"s_waitcnt vmcnt(2)\n"
"v_max3_u32 v105, v31, v32, v33\n"
"v_max3_u32 v104, v104, v34, v35\n"
"v_mfma_f32_16x16x32_bf16 v[100:103], v[32:35], v[24:27], v[100:103]\n"
"s_waitcnt vmcnt(1)\n"
"v_max3_u32 v105, v105, v36, v37\n"
"v_max3_u32 v104, v104, v38, v39\n"
"v_mfma_f32_16x16x32_bf16 v[100:103], v[36:39], v[24:27], v[100:103]\n"
"s_waitcnt vmcnt(0)\n"
"v_mfma_f32_16x16x32_bf16 v[100:103], v[40:43], v[24:27], v[100:103]\n"
"v_max3_u32 v105, v105, v40, v41\n"
"v_max3_u32 v104, v104, v42, v43\n"
"v_max_u32 v104, v104, v105\n"
"v_cmp_eq_u32 vcc, -1, v104\n"
"s_cbranch_vccz LgotP%=\n"
"s_nop 3\n"
"s_sub_u32 s55, s55, 1\n"
"s_cmp_lg_u32 s55, 0\n"
"s_cbranch_scc1 LpollP%=\n"
"s_mov_b32 s52, 4\n"
"s_branch Lfail%=\n"
"LgotP%=:\n"
"s_nop 3\n"
"v_permlane32_swap_b32 v100, v102\n"
"v_permlane32_swap_b32 v101, v103\n"
"v_pk_add_f32 v[100:101], v[100:101], v[102:103]\n"
"v_cvt_pk_bf16_f32 v124, v100, v101\n"
"v_min_u32 v124, s57, v124\n"
"s_mov_b32 exec_lo, 0x00ff00ff\n"
"s_mov_b32 exec_hi, 0x00ff00ff\n"
"buffer_store_dword v124, v10, s[36:39], s62 offen sc1\n"
"s_mov_b64 exec, -1\n"
"s_branch Lnext%=\n"
"Lside%=:\n"
"s_cmp_eq_u32 %[w], 3\n"
"s_cbranch_scc0 Lnext%=\n"
"s_cmp_eq_u32 %[g], 0\n"
"s_cbranch_scc0 Lnext%=\n"
"ds_read_b64_tr_b16 v[28:29], v5\n"
"ds_read_b64_tr_b16 v[30:31], v5 offset:512\n"
"ds_read_b64_tr_b16 v[32:33], v5 offset:1024\n"
"ds_read_b64_tr_b16 v[34:35], v5 offset:1536\n"
"ds_read_b64_tr_b16 v[36:37], v5 offset:2048\n"
"ds_read_b64_tr_b16 v[38:39], v5 offset:2560\n"
"ds_read_b64_tr_b16 v[40:41], v5 offset:3072\n"
"ds_read_b64_tr_b16 v[42:43], v5 offset:3584\n"
"s_waitcnt lgkmcnt(0)\n"
"s_mov_b32 exec_lo, 0x00ff00ff\n"
"s_mov_b32 exec_hi, 0x00ff00ff\n"
"buffer_store_dwordx4 v[28:31], v11, s[36:39], s49 offen sc1\n"
"buffer_store_dwordx4 v[32:35], v11, s[36:39], s49 offen offset:512 sc1\n"
"buffer_store_dwordx4 v[36:39], v11, s[36:39], s49 offen offset:1024 sc1\n"
"buffer_store_dwordx4 v[40:43], v11, s[36:39], s49 offen offset:1536 sc1\n"
"s_mov_b64 exec, -1\n"
"s_cmp_eq_u32 s44, 0\n"
"s_cbranch_scc1 Lnext%=\n"
"s_sub_u32 s52, s44, 1\n"
"v_mov_b32 v127, s52\n"
"buffer_store_dword v127, off, s[36:39], s58 sc1\n"
"Lnext%=:\n"
"s_add_u32 s49, s49, 65536\n"
"s_add_u32 s50, s50, 65536\n"
"s_mov_b32 s45, s62\n"
"s_xor_b32 s46, s46, 524288\n"
"s_add_u32 s44, s44, 1\n"
"s_branch Lstep%=\n"
"Llast%=:\n"
"s_cmp_eq_u32 %[g], 0\n"
"s_cbranch_scc0 Lend%=\n"
"s_cmp_eq_u32 %[w], 3\n"
"s_cbranch_scc0 Llast2%=\n"
"ds_read_b64_tr_b16 v[28:29], v5\n"
"ds_read_b64_tr_b16 v[30:31], v5 offset:512\n"
"ds_read_b64_tr_b16 v[32:33], v5 offset:1024\n"
"ds_read_b64_tr_b16 v[34:35], v5 offset:1536\n"
"ds_read_b64_tr_b16 v[36:37], v5 offset:2048\n"
"ds_read_b64_tr_b16 v[38:39], v5 offset:2560\n"
"ds_read_b64_tr_b16 v[40:41], v5 offset:3072\n"
"ds_read_b64_tr_b16 v[42:43], v5 offset:3584\n"
"s_waitcnt lgkmcnt(0)\n"
"s_mov_b32 exec_lo, 0x00ff00ff\n"
"s_mov_b32 exec_hi, 0x00ff00ff\n"
"buffer_store_dwordx4 v[28:31], v11, s[36:39], s49 offen sc1\n"
"buffer_store_dwordx4 v[32:35], v11, s[36:39], s49 offen offset:512 sc1\n"
"buffer_store_dwordx4 v[36:39], v11, s[36:39], s49 offen offset:1024 sc1\n"
"buffer_store_dwordx4 v[40:43], v11, s[36:39], s49 offen offset:1536 sc1\n"
"s_mov_b64 exec, -1\n"
"Llast2%=:\n"
"s_waitcnt vmcnt(0)\n"
"s_barrier\n"
"s_cmp_eq_u32 %[w], 3\n"
"s_cbranch_scc0 Lend%=\n"
"v_mov_b32 v127, 63\n"
"buffer_store_dword v127, off, s[36:39], s58 sc1\n"
"s_branch Lend%=\n"
"Lfail%=:\n"
"s_mov_b64 exec, -1\n"
"v_mov_b32 v127, s52\n"
"s_mov_b32 s53, 2098432\n"
"buffer_store_dword v127, off, s[36:39], s53 sc1\n"
"Lend%=:\n"
"s_waitcnt vmcnt(0)\n"
      :
      : [base_lo] "s"((unsigned)pb), [base_hi] "s"((unsigned)(pb >> 32)), [wq_lo] "s"((unsigned)pq), [wq_hi] "s"((unsigned)(pq >> 32)),
        [wk_lo] "s"((unsigned)pk), [wk_hi] "s"((unsigned)(pk >> 32)), [wv_lo] "s"((unsigned)pv), [wv_hi] "s"((unsigned)(pv >> 32)),
        [wo_lo] "s"((unsigned)po), [wo_hi] "s"((unsigned)(po >> 32)), [mem0_lo] "s"((unsigned)pm), [mem0_hi] "s"((unsigned)(pm >> 32)),
        [h] "s"(h), [g] "s"(g), [w] "s"(w), [d1] "s"(p.d1), [d2] "s"(p.d2), [kprog] "s"(kprog), [vprog] "s"(vprog)
      : "memory", "vcc", "scc",
"s36", "s37", "s38", "s39", "s40", "s41", "s42", "s43", "s44", "s45", "s46", "s47", "s48", "s49", "s50", "s51",
"s52", "s53", "s54", "s55", "s56", "s57", "s58", "s59", "s60", "s61", "s62", "s63", "s64", "s65", "s66", "s67",
"s68", "s69", "s70", "s71", "v0", "v1", "v2", "v3", "v4", "v5", "v6", "v7", "v8", "v9", "v10", "v11",
"v12", "v13", "v14", "v15", "v16", "v17", "v18", "v19", "v20", "v21", "v22", "v23", "v24", "v25", "v26", "v27",
"v28", "v29", "v30", "v31", "v32", "v33", "v34", "v35", "v36", "v37", "v38", "v39", "v40", "v41", "v42", "v43",
"v44", "v45", "v46", "v47", "v48", "v49", "v50", "v51", "v52", "v53", "v54", "v55", "v56", "v57", "v58", "v59",
"v60", "v61", "v62", "v63", "v64", "v65", "v66", "v67", "v68", "v69", "v70", "v71", "v72", "v73", "v74", "v75",
"v76", "v77", "v78", "v79", "v80", "v81", "v82", "v83", "v84", "v85", "v86", "v87", "v88", "v89", "v90", "v91",
"v92", "v93", "v94", "v95", "v96", "v97", "v98", "v99", "v100", "v101", "v102", "v103", "v104", "v105", "v106", "v107",
"v108", "v109", "v110", "v111", "v112", "v113", "v114", "v115", "v116", "v117", "v118", "v119", "v120", "v121", "v122", "v123",
"v124", "v125", "v126", "v127", "v128", "v129", "v130", "v131", "v132", "v133", "v134", "v135", "v136", "v137", "v138", "v139",
"v140", "v141", "v142", "v143", "v144", "v145", "v146", "v147", "v148", "v149", "v150", "v151", "v152", "v153", "v154", "v155",
"v156", "v157", "v158", "v159", "v160", "v161", "v162", "v163", "v164", "v165", "v166", "v167", "v168", "v169", "v170", "v171",
"v172", "v173", "v174", "v175", "v176", "v177", "v178", "v179", "v180", "v181", "v182", "v183", "v184", "v185", "v186", "v187",
"v188", "v189", "v190", "v191", "v192", "v193", "v194", "v195", "v196", "v197", "v198", "v199", "v200", "v201", "v202", "v203",
"v204", "v205", "v206", "v207", "v208", "v209", "v210", "v211", "v212", "v213", "v214", "v215", "v216", "v217", "v218", "v219",
"v220", "v221", "v222", "v223", "v224", "v225", "v226", "v227", "v228", "v229", "v230", "v231", "v232", "v233", "v234", "v235",
"v236", "v237", "v238", "v239", "v240", "v241", "v242", "v243", "v244", "v245", "v246", "v247", "v248", "v249", "v250", "v251",
"v252", "v253", "v254", "v255", "a0", "a1", "a2", "a3", "a4", "a5", "a6", "a7", "a8", "a9", "a10", "a11",
"a12", "a13", "a14", "a15", "a16", "a17", "a18", "a19", "a20", "a21", "a22", "a23", "a24", "a25", "a26", "a27",
"a28", "a29", "a30", "a31", "a32", "a33", "a34", "a35", "a36", "a37", "a38", "a39", "a40", "a41", "a42", "a43",
"a44", "a45", "a46", "a47", "a48", "a49", "a50", "a51", "a52", "a53", "a54", "a55", "a56", "a57", "a58", "a59",
"a60", "a61", "a62", "a63", "a64", "a65", "a66", "a67", "a68", "a69", "a70", "a71", "a72", "a73", "a74", "a75",
"a76", "a77", "a78", "a79", "a80", "a81", "a82", "a83", "a84", "a85", "a86", "a87", "a88", "a89", "a90", "a91",
"a92", "a93", "a94", "a95", "a96", "a97", "a98", "a99", "a100", "a101", "a102", "a103", "a104", "a105", "a106", "a107",
"a108", "a109", "a110", "a111", "a112", "a113", "a114", "a115", "a116", "a117", "a118", "a119", "a120", "a121", "a122", "a123",
"a124", "a125", "a126", "a127", "a128", "a129", "a130", "a131", "a132", "a133", "a134", "a135", "a136", "a137", "a138", "a139",
"a140", "a141", "a142", "a143", "a144", "a145", "a146", "a147", "a148", "a149", "a150", "a151", "a152", "a153", "a154", "a155",
"a156", "a157", "a158", "a159", "a160", "a161", "a162", "a163", "a164", "a165", "a166", "a167", "a168", "a169", "a170", "a171",
"a172", "a173", "a174", "a175", "a176", "a177", "a178", "a179", "a180", "a181", "a182", "a183", "a184", "a185", "a186", "a187",
"a188", "a189", "a190", "a191", "a192", "a193", "a194", "a195", "a196", "a197", "a198", "a199", "a200", "a201", "a202", "a203",
"a204", "a205", "a206", "a207", "a208", "a209", "a210", "a211", "a212", "a213", "a214", "a215", "a216", "a217", "a218", "a219",
"a220", "a221", "a222", "a223", "a224", "a225", "a226", "a227", "a228", "a229", "a230", "a231", "a232", "a233", "a234", "a235",
"a236", "a237", "a238", "a239", "a240", "a241", "a242", "a243", "a244", "a245", "a246", "a247", "a248", "a249", "a250", "a251",
"a252", "a253", "a254", "a255"
  );
}

template <int KIND>
__device__ __forceinline__ void proj_role(const Params& p, int sig, int w, int lane) {
  const int half = lane >> 5, pi = lane & 31;
  const char* src[2];
  size_t rstride;
#pragma unroll
  for (int par = 0; par < 2; ++par) {
    const int f = half | ((w & 1) << 1) | (par << 2), chi = pi ^ (f << 1);
    if (KIND != 2) {
      const u16* m = sig < 16 ? p.wq : (sig < 32 ? p.wk : p.wv);
      src[par] = (const char*)(m + (size_t)(2 * w + half) * 4096 + (sig & 15) * 256 + chi * 8);
    } else {
      const u16* m = chi < 16 ? p.w1 : p.w3;
      src[par] = (const char*)(m + (size_t)(2 * w + half) * 1792 + (sig - 48) * 128 + (chi & 15) * 8);
    }
  }
  rstride = KIND != 2 ? 8192 : 3584;
  auto issue = [&](int ci) {
#pragma unroll
    for (int j = 0; j < 8; ++j) dma_ptr((ci & 3) * 32768 + j * 4096 + w * 1024, src[j & 1] + (size_t)(ci * 64 + 8 * j) * rstride);
  };
  const int g = lane >> 4, q = (lane >> 2) & 3, pp = lane & 3;
  const int ftr = q | ((g & 1) << 2);
  unsigned tra[4];
#pragma unroll
  for (int t = 0; t < 4; ++t) {
    const int chi0 = KIND != 2 ? 8 * w + 2 * t : (t >> 1) * 16 + 4 * w + 2 * (t & 1);
    tra[t] = (8 * g + q) * 512 + ((chi0 ^ (ftr << 1)) + (pp >> 1)) * 16 + (pp & 1) * 8;
  }
  v4u wf[64];
  issue(0); issue(1); issue(2);
#pragma unroll
  for (int ci = 0; ci < 8; ++ci) {
    if (ci < 6) asm volatile("s_waitcnt vmcnt(16)" ::: "memory");
    else if (ci == 6) asm volatile("s_waitcnt vmcnt(8)" ::: "memory");
    else wait_vm0();
    bar();
    if (ci + 3 < 8) issue(ci + 3);
#pragma unroll
    for (int t = 0; t < 4; ++t)
#pragma unroll
      for (int kl = 0; kl < 2; ++kl) {
        const unsigned a = (ci & 3) * 32768 + tra[t] + kl * 32 * 512;
        const v2u lo = lds_tr(a), hi = lds_tr(a + 4 * 512);
        wf[t * 16 + ci * 2 + kl] = (v4u){lo.x, lo.y, hi.x, hi.y};
      }
    wait_lgkm0();
  }
  bar();

  constexpr unsigned XROW = 1056, XBUF = 32 * XROW;
  const v4u xsrd = make_srd(p.x, 2048 * 1024);
  const __amdgpu_buffer_rsrc_t ws = ws_rsrc(p);
  auto xload = [&](int seg) {
#pragma unroll
    for (int i = 0; i < 8; ++i) dma_buf((seg & 1) * XBUF + (8 * w + i) * XROW, lane * 16, xsrd, (seg * 32 + 8 * w + i) * 1024);
  };
  xload(0);
  wait_vm0();
  bar();
  const unsigned xa = (lane & 15) * XROW + g * 16;
  constexpr int NST = KIND == 2 ? 2 : 4;
  for (int seg = 0; seg < 64; ++seg) {
    const unsigned xb = xa + (seg & 1) * XBUF;
    f32x4 acc[2][4];
    v4u x0 = lds_ld16(xb), x1 = lds_ld16(xb + 16 * XROW);
#pragma unroll
    for (int ks = 0; ks < 16; ++ks) {
      v4u n0 = x0, n1 = x1;
      if (ks < 15) { n0 = lds_ld16(xb + (ks + 1) * 64); n1 = lds_ld16(xb + 16 * XROW + (ks + 1) * 64); }
      if (seg + 1 < 64 && (ks & 1) == 0) dma_buf(((seg + 1) & 1) * XBUF + (8 * w + (ks >> 1)) * XROW, lane * 16, xsrd, ((seg + 1) * 32 + 8 * w + (ks >> 1)) * 1024);
#pragma unroll
      for (int t = 0; t < 4; ++t) {
        if (KIND == 1) {
          if (ks == 0) { mfma_wb0(acc[0][t], wf[t * 16], x0); mfma_wb0(acc[1][t], wf[t * 16], x1); }
          else { mfma_wb(acc[0][t], wf[t * 16 + ks], x0); mfma_wb(acc[1][t], wf[t * 16 + ks], x1); }
        } else {
          if (ks == 0) { mfma_wa0(acc[0][t], wf[t * 16], x0); mfma_wa0(acc[1][t], wf[t * 16], x1); }
          else { mfma_wa(acc[0][t], wf[t * 16 + ks], x0); mfma_wa(acc[1][t], wf[t * 16 + ks], x1); }
        }
      }
      x0 = n0; x1 = n1;
    }
    mfma_settle(acc[0][0], acc[1][0]); mfma_settle(acc[0][1], acc[1][1]); mfma_settle(acc[0][2], acc[1][2]); mfma_settle(acc[0][3], acc[1][3]);
    if (KIND == 0) {
      const unsigned o = (sig < 16 ? OFF_QX : OFF_KX) + (unsigned)(seg * 32 + (sig & 15) * 2 + (w >> 1)) * 8192 + (w & 1) * 2048;
#pragma unroll
      for (int tx = 0; tx < 2; ++tx)
#pragma unroll
        for (int j = 0; j < 2; ++j) {
          const f32x4 a = acc[tx][2 * j], b = acc[tx][2 * j + 1];
          st16(ws, (v4u){pk2(a[0], a[1]), pk2(a[2], a[3]), pk2(b[0], b[1]), pk2(b[2], b[3])}, lane * 16, o + tx * 4096 + j * 1024);
        }
    } else if (KIND == 1) {
      const unsigned o = OFF_VX + (unsigned)(seg * 32 + (sig & 15) * 2 + (w >> 1)) * 8192 + (w & 1) * 4096;
#pragma unroll
      for (int t = 0; t < 4; ++t) {
        const f32x4 a = acc[0][t], b = acc[1][t];
        st16(ws, (v4u){pk2(a[0], a[1]), pk2(a[2], a[3]), pk2(b[0], b[1]), pk2(b[2], b[3])}, lane * 16, o + t * 1024);
      }
    } else {
#pragma unroll
      for (int tx = 0; tx < 2; ++tx) {
        float hv[8];
#pragma unroll
        for (int t = 0; t < 2; ++t)
#pragma unroll
          for (int r = 0; r < 4; ++r) {
            const float gt = acc[tx][t][r];
            hv[t * 4 + r] = gt * __builtin_amdgcn_rcpf(1.0f + __expf(-gt)) * acc[tx][2 + t][r];
          }
        st16(ws, (v4u){pk2(hv[0], hv[1]), pk2(hv[2], hv[3]), pk2(hv[4], hv[5]), pk2(hv[6], hv[7])}, lane * 16,
             OFF_HB + (unsigned)((seg * 2 + tx) * 56 + (sig - 48) * 4 + w) * 1024);
      }
    }
    if (seg + 1 < 64) asm volatile("s_waitcnt vmcnt(%0)" ::"i"(NST) : "memory");
    else wait_vm0();
    bar();
    if (w == 0 && lane == 0) {
      if (seg + 1 == 64) st_agent(p.base, CT_PROG + sig * 128, 63u);
      else if (seg > 0) st_agent(p.base, CT_PROG + sig * 128, (unsigned)(seg - 1));
    }
  }
}

template <int NT, int KS, int PASS>
__device__ __forceinline__ void load_stationary(v4u (&wf)[NT * KS], const u16* wcol, int w, int lane) {
  constexpr int ROWB = NT * 32, RPI = 1024 / ROWB, LPR = ROWB / 16;
  constexpr int NPASS = (KS + PASS - 1) / PASS;
  const unsigned reg = w * 32768;
  const char* src = (const char*)(wcol + (size_t)(KS * 32 * w + lane / LPR) * 512) + (lane % LPR) * 16;
  const int g = lane >> 4, q = (lane >> 2) & 3, pp = lane & 3;
  const unsigned tra = reg + (4 * g + q) * ROWB + pp * 8;
#pragma unroll
  for (int ps = 0; ps < NPASS; ++ps) {
    const int k0 = ps * PASS, kn = (KS - k0 < PASS) ? KS - k0 : PASS;
#pragma unroll
    for (int i = 0; i < PASS * 32 / RPI; ++i)
      if (i < kn * 32 / RPI) dma_ptr(reg + i * 1024, src + (size_t)(k0 * 32 + i * RPI) * 1024);
    wait_vm0();
#pragma unroll
    for (int kl = 0; kl < PASS; ++kl)
      if (kl < kn) {
#pragma unroll
        for (int t = 0; t < NT; ++t) {
          const unsigned a = tra + kl * 32 * ROWB + t * 32;
          const v2u lo = lds_tr(a), hi = lds_tr(a + 16 * ROWB);
          wf[t * KS + k0 + kl] = (v4u){lo.x, lo.y, hi.x, hi.y};
        }
      }
    wait_lgkm0();
  }
}

constexpr unsigned L_RED = 131072, L_FLAG = 147456;

__device__ __forceinline__ void down_role(const Params& p, int cs, int tx, int w, int lane) {
  constexpr int NT = 4, KS = 14;
  v4u wf[NT * KS];
  load_stationary<NT, KS, 8>(wf, p.w2 + cs * 64, w, lane);
  const __amdgpu_buffer_rsrc_t ws = ws_rsrc(p);
  const int tid = w * 64 + lane;
  for (int seg = 0; seg < 64; ++seg) {
    if (!wg_wait(p, w, lane < 14 ? CT_PROG + (48 + lane) * 128 : ~0u, seg, 10, 2, L_FLAG)) return;
    const unsigned src = OFF_HB + (unsigned)((seg * 2 + tx) * 56 + KS * w) * 1024;
    v4u xf[KS];
#pragma unroll
    for (int k = 0; k < KS; ++k) xf[k] = ld16(ws, lane * 16, src + k * 1024);
    f32x4 acc[NT];
#pragma unroll
    for (int k = 0; k < KS; ++k)
#pragma unroll
      for (int t = 0; t < NT; ++t) { if (k == 0) mfma_wa0(acc[t], wf[t * KS], xf[0]); else mfma_wa(acc[t], wf[t * KS + k], xf[k]); }
    mfma_settle(acc[0], acc[1]); mfma_settle(acc[2], acc[3]);
#pragma unroll
    for (int t = 0; t < NT; ++t) lds_st16(L_RED + (w * NT + t) * 1024 + lane * 16, __builtin_bit_cast(v4u, acc[t]));
    wait_lgkm0();
    bar();
    {
      const int tok = tid >> 4, cg = tid & 15;
      const unsigned a = L_RED + (cg >> 2) * 1024 + ((cg & 3) * 16 + tok) * 16;
      f32x4 s = __builtin_bit_cast(f32x4, lds_ld16(a));
#pragma unroll
      for (int u = 1; u < 4; ++u) s += __builtin_bit_cast(f32x4, lds_ld16(a + u * NT * 1024));
      st16(ws, __builtin_bit_cast(v4u, s), (unsigned)(tok * 2048 + cg * 16), OFF_BB + (unsigned)(seg * 32 + tx * 16) * 2048 + cs * 256);
    }
    wait_vm0();
    bar();
    if (tid == 0) st_agent(p.base, CT_DPROG + (cs * 2 + tx) * 128, (unsigned)seg);
  }
}

__device__ __forceinline__ void out_role(const Params& p, int cs, int tx, int w, int lane) {
  constexpr int NT = 2, KS = 32;
  v4u wf[NT * KS];
  load_stationary<NT, KS, 16>(wf, p.wo + cs * 32, w, lane);
  const __amdgpu_buffer_rsrc_t ws = ws_rsrc(p);
  if (!wg_wait(p, w, lane == 0 ? CT_GCNT : ~0u, 3, 20, 2, L_FLAG)) return;
  for (int seg = 0; seg < 64; ++seg) {
    {
      bool ok = true;
      if (w == 0) {
        ok = wait_words(p, lane == 0 ? CT_DPROG + ((cs >> 1) * 2 + tx) * 128 : ~0u, seg, 21, 2) &&
             wait_words(p, lane == 0 ? CT_OCNT + seg * 128 : ~0u, 31, 22, 1);
        *(volatile LAS unsigned*)(size_t)L_FLAG = ok ? 1u : 0u;
        wait_lgkm0();
      }
      bar();
      const unsigned v = *(volatile LAS unsigned*)(size_t)L_FLAG;
      wait_lgkm0();
      if (rfl(v) == 0) return;
    }
    const unsigned src = OFF_OX + (unsigned)((seg * 2 + tx) * 128 + KS * w) * 1024;
    v4u xf[KS];
#pragma unroll
    for (int k = 0; k < KS; ++k) xf[k] = ld16(ws, lane * 16, src + k * 1024);
    const int tok = lane >> 2, c8 = lane & 3;
    const unsigned row = seg * 32 + tx * 16 + tok;
    f32x4 b0, b1; float gate = 0.f;
    if (w == 0) {
      b0 = __builtin_bit_cast(f32x4, ld16(ws, row * 2048 + cs * 128 + c8 * 32, OFF_BB));
      b1 = __builtin_bit_cast(f32x4, ld16(ws, row * 2048 + cs * 128 + c8 * 32 + 16, OFF_BB));
      gate = __uint_as_float(__builtin_amdgcn_raw_buffer_load_b32(ws, row * 4, OFF_GG, 16));
    }
    f32x4 acc[NT];
#pragma unroll
    for (int k = 0; k < KS; ++k)
#pragma unroll
      for (int t = 0; t < NT; ++t) { if (k == 0) mfma_wa0(acc[t], wf[t * KS], xf[0]); else mfma_wa(acc[t], wf[t * KS + k], xf[k]); }
    mfma_settle(acc[0], acc[1]);
#pragma unroll
    for (int t = 0; t < NT; ++t) lds_st16(L_RED + (w * NT + t) * 1024 + lane * 16, __builtin_bit_cast(v4u, acc[t]));
    wait_lgkm0();
    bar();
    if (w == 0) {
      const unsigned a = L_RED + (c8 >> 1) * 1024 + ((c8 & 1) * 32 + tok) * 16;
      f32x4 s0 = __builtin_bit_cast(f32x4, lds_ld16(a)), s1 = __builtin_bit_cast(f32x4, lds_ld16(a + 256));
#pragma unroll
      for (int u = 1; u < 4; ++u) {
        s0 += __builtin_bit_cast(f32x4, lds_ld16(a + u * NT * 1024));
        s1 += __builtin_bit_cast(f32x4, lds_ld16(a + u * NT * 1024 + 256));
      }
      const float g1 = 1.0f - gate;
      s0 = gate * s0 + g1 * b0; s1 = gate * s1 + g1 * b1;
      *(GAS v4u*)(p.out + (size_t)row * 512 + cs * 32 + c8 * 8) = (v4u){pk2(s0[0], s0[1]), pk2(s0[2], s0[3]), pk2(s1[0], s1[1]), pk2(s1[2], s1[3])};
      wait_lgkm0();
    }
    bar();
  }
}

__device__ __forceinline__ f32x4 mma32(v4u a, v4u b, f32x4 c) {
  return __builtin_amdgcn_mfma_f32_16x16x32_bf16(__builtin_bit_cast(bf16x8, a), __builtin_bit_cast(bf16x8, b), c, 0, 0, 0);
}
__device__ __forceinline__ f32x4 mma16(v2u a, v2u b, f32x4 c) {
  return __builtin_amdgcn_mfma_f32_16x16x16bf16_1k(__builtin_bit_cast(s16x4, a), __builtin_bit_cast(s16x4, b), c, 0, 0, 0);
}
__device__ __forceinline__ float xmax(float v) { v = fmaxf(v, __shfl_xor(v, 16)); return fmaxf(v, __shfl_xor(v, 32)); }

__device__ __forceinline__ void attn_role(const Params& p, int h, int par, int lane) {
  const __amdgpu_buffer_rsrc_t ws = ws_rsrc(p);
  const int c = lane & 15, g = lane >> 4;
  const float NEG = -__builtin_inff();
  const float sc = 0.12751743082459868f;
  const unsigned pw = lane < 3 ? CT_PROG + (lane * 16 + (h >> 1)) * 128 : ~0u;
  const f32x4 zero = {0.f, 0.f, 0.f, 0.f};
  const v4u ones = {0x3f803f80u, 0x3f803f80u, 0x3f803f80u, 0x3f803f80u};
  for (int seg = par; seg < 64; seg += 2) {
    if (!wait_words(p, pw, seg, 30, 4)) return;
    const unsigned item = (unsigned)(seg * 32 + h);
    v4u qf[2][4], kf[2][4], vf[8];
#pragma unroll
    for (int t = 0; t < 2; ++t)
#pragma unroll
      for (int ks = 0; ks < 4; ++ks) {
        qf[t][ks] = ld16(ws, lane * 16, OFF_QX + item * 8192 + (t * 4 + ks) * 1024);
        kf[t][ks] = ld16(ws, lane * 16, OFF_KX + item * 8192 + (t * 4 + ks) * 1024);
      }
#pragma unroll
    for (int e = 0; e < 8; ++e) vf[e] = ld16(ws, lane * 16, OFF_VX + item * 8192 + e * 1024);
    f32x4 s00 = zero, s01 = zero, s11 = zero;
#pragma unroll
    for (int ks = 0; ks < 4; ++ks) {
      s00 = mma32(kf[0][ks], qf[0][ks], s00);
      s01 = mma32(kf[0][ks], qf[1][ks], s01);
      s11 = mma32(kf[1][ks], qf[1][ks], s11);
    }
    if (!wait_words(p, lane == 0 ? CT_FM + h * 128 : ~0u, seg, 31, 2)) return;
    v4u km[4]; v2u vm[8];
#pragma unroll
    for (int ks = 0; ks < 4; ++ks) km[ks] = ld16(ws, g * 128 + (c & 7) * 16, OFF_KM + item * 2048 + ks * 512);
#pragma unroll
    for (int e = 0; e < 8; ++e) vm[e] = __builtin_amdgcn_raw_buffer_load_b64(ws, (g & 1) * 128 + c * 8, OFF_VM + item * 2048 + e * 256, 16);
    f32x4 sm0 = zero, sm1 = zero;
#pragma unroll
    for (int ks = 0; ks < 4; ++ks) { sm0 = mma32(km[ks], qf[0][ks], sm0); sm1 = mma32(km[ks], qf[1][ks], sm1); }
#pragma unroll
    for (int r = 0; r < 4; ++r) {
      const bool vis = 4 * g + r <= c;
      s00[r] = vis ? s00[r] : NEG;
      s11[r] = vis ? s11[r] : NEG;
      sm0[r] = g < 2 ? sm0[r] : NEG;
      sm1[r] = g < 2 ? sm1[r] : NEG;
    }
    float m0 = fmaxf(fmaxf(fmaxf(s00[0], s00[1]), fmaxf(s00[2], s00[3])), fmaxf(fmaxf(sm0[0], sm0[1]), fmaxf(sm0[2], sm0[3])));
    float m1 = fmaxf(fmaxf(fmaxf(s01[0], s01[1]), fmaxf(s01[2], s01[3])), fmaxf(fmaxf(sm1[0], sm1[1]), fmaxf(sm1[2], sm1[3])));
    m1 = fmaxf(m1, fmaxf(fmaxf(s11[0], s11[1]), fmaxf(s11[2], s11[3])));
    m0 = xmax(m0) * sc; m1 = xmax(m1) * sc;
#pragma unroll
    for (int r = 0; r < 4; ++r) {
      s00[r] = __builtin_amdgcn_exp2f(s00[r] * sc - m0); sm0[r] = __builtin_amdgcn_exp2f(sm0[r] * sc - m0);
      s01[r] = __builtin_amdgcn_exp2f(s01[r] * sc - m1); s11[r] = __builtin_amdgcn_exp2f(s11[r] * sc - m1);
      sm1[r] = __builtin_amdgcn_exp2f(sm1[r] * sc - m1);
    }
    const v4u px0 = {pk2(s00[0], s00[1]), pk2(s00[2], s00[3]), 0u, 0u};
    const v4u px1 = {pk2(s01[0], s01[1]), pk2(s01[2], s01[3]), pk2(s11[0], s11[1]), pk2(s11[2], s11[3])};
    const v2u pm0 = {pk2(sm0[0], sm0[1]), pk2(sm0[2], sm0[3])}, pm1 = {pk2(sm1[0], sm1[1]), pk2(sm1[2], sm1[3])};
    const v2u ones2 = {ones.x, ones.y};
    const float r0 = __builtin_amdgcn_rcpf(mma16(ones2, pm0, mma32(ones, px0, zero))[0]);
    const float r1 = __builtin_amdgcn_rcpf(mma16(ones2, pm1, mma32(ones, px1, zero))[0]);
#pragma unroll
    for (int ks = 0; ks < 4; ++ks) {
      const f32x4 a0 = mma16(vm[2 * ks], pm0, mma32(vf[2 * ks], px0, zero)) * r0, b0 = mma16(vm[2 * ks + 1], pm0, mma32(vf[2 * ks + 1], px0, zero)) * r0;
      const f32x4 a1 = mma16(vm[2 * ks], pm1, mma32(vf[2 * ks], px1, zero)) * r1, b1 = mma16(vm[2 * ks + 1], pm1, mma32(vf[2 * ks + 1], px1, zero)) * r1;
      st16(ws, (v4u){pk2(a0[0], a0[1]), pk2(a0[2], a0[3]), pk2(b0[0], b0[1]), pk2(b0[2], b0[3])}, lane * 16, OFF_OX + (unsigned)((seg * 2) * 128 + h * 4 + ks) * 1024);
      st16(ws, (v4u){pk2(a1[0], a1[1]), pk2(a1[2], a1[3]), pk2(b1[0], b1[1]), pk2(b1[2], b1[3])}, lane * 16, OFF_OX + (unsigned)((seg * 2 + 1) * 128 + h * 4 + ks) * 1024);
    }
    wait_vm0();
    if (lane == 0) add_agent(p.base, CT_OCNT + seg * 128);
  }
}

__device__ __forceinline__ void gate_role(const Params& p, int id, int w, int lane) {
  const v4u gw = *(const GAS v4u*)(p.gw + lane * 8);
  const __amdgpu_buffer_rsrc_t ws = ws_rsrc(p);
  for (int r0 = id * 512 + w * 128; r0 < id * 512 + w * 128 + 128; r0 += 8) {
    v4u xr[8];
#pragma unroll
    for (int i = 0; i < 8; ++i) xr[i] = *(const GAS v4u*)(p.x + (size_t)(r0 + i) * 512 + lane * 8);
    float mine = 0.f;
#pragma unroll
    for (int i = 0; i < 8; ++i) {
      float s = 0.f;
#pragma unroll
      for (int j = 0; j < 4; ++j)
        s += __uint_as_float(xr[i][j] << 16) * __uint_as_float(gw[j] << 16) + __uint_as_float(xr[i][j] & 0xffff0000u) * __uint_as_float(gw[j] & 0xffff0000u);
#pragma unroll
      for (int m = 32; m >= 1; m >>= 1) s += __shfl_xor(s, m);
      if (lane == i) mine = s;
    }
    if (lane < 8) __builtin_amdgcn_raw_buffer_store_b32(__float_as_uint(__builtin_amdgcn_rcpf(1.0f + __expf(-mine))), ws, (r0 + lane) * 4, OFF_GG, 16);
  }
  wait_vm0();
  bar();
  if (w == 0 && lane == 0) add_agent(p.base, CT_GCNT);
}

__global__ void __launch_bounds__(256, 1) mega(Params p) {
  extern __shared__ __attribute__((aligned(16))) char smem[];
  const int tid = threadIdx.x, lane = tid & 63;
  const int w = rfl(tid >> 6);
  if (tid == 0) {
    const unsigned xcc = xcc_id() & 7;
    const unsigned t = __hip_atomic_fetch_add((GAS unsigned*)(p.base + CT_XCNT + xcc * 128), 1u, RLX_AGENT) + 1u;
    unsigned slot = xcc * 32 + t;
    if (t >= 32) {
      const unsigned rank = __hip_atomic_fetch_add((GAS unsigned*)(p.base + CT_OVF), 1u, RLX_AGENT) + 1u;
      slot = ~0u;
      for (int s = 0; s < SPIN_CAP && slot == ~0u; ++s) {
        unsigned total = 0;
        for (int x = 0; x < 8; ++x) total += ld_agent(p.base, CT_XCNT + x * 128) + 1u;
        if (total < gridDim.x) continue;
        unsigned left = rank;
        for (int x = 0; x < 8 && slot == ~0u; ++x) {
          const unsigned cnt = ld_agent(p.base, CT_XCNT + x * 128) + 1u, miss = cnt < 32 ? 32 - cnt : 0;
          if (left < miss) slot = x * 32 + cnt + left; else left -= miss;
        }
        break;
      }
    }
    *(volatile LAS unsigned*)(size_t)L_FLAG = slot;
  }
  __syncthreads();
  const unsigned slot = rfl(*(volatile LAS unsigned*)(size_t)L_FLAG);
  __syncthreads();
  if (slot >= 256) return;
  const int xcc = slot >> 5, t = slot & 31;
  if (xcc < 4) {
    if (t < 16) { if (p.mask & 1) head_role(p, xcc * 8 + (t >> 1), t & 1, w); return; }
    if (t < 20) { if (p.mask & 2) { const int a = (xcc * 4 + t - 16) * 4 + w; attn_role(p, a & 31, a >> 5, lane); } return; }
    if (xcc == 0 && t == 20 && tid == 0) {
      const unsigned a = probe_rt(p.base + 2048) + probe_rt(p.base + 16384 + 4096 + 2048);
      const unsigned b = probe_rt(p.base + 8192 + 2048) + probe_rt(p.base + 16384 + 8192 + 4096 + 2048);
      st_agent(p.base, CT_CVAL, a <= b ? 0u : 1u);
    }
    return;
  }
  if (xcc < 6) {
    const int sig = (xcc - 4) * 32 + t;
    if (!(p.mask & 4)) return;
    if (sig < 32) proj_role<0>(p, sig, w, lane);
    else if (sig < 48) proj_role<1>(p, sig, w, lane);
    else if (sig < 62) proj_role<2>(p, sig, w, lane);
    return;
  }
  const int tx = xcc - 6;
  if (t < 16) { if (p.mask & 16) out_role(p, t, tx, w, lane); }
  else if (t < 24) { if (p.mask & 8) down_role(p, t - 16, tx, w, lane); }
  else if (t < 26) { if (p.mask & 32) gate_role(p, tx * 2 + t - 24, w, lane); }
}

static Params make_params(void* const* d_in, void* d_out, void* d_ws) {
  Params p;
  p.x = (const u16*)d_in[0]; p.mem0 = (const u16*)d_in[1]; p.wq = (const u16*)d_in[2]; p.wk = (const u16*)d_in[3];
  p.wv = (const u16*)d_in[4]; p.wo = (const u16*)d_in[5]; p.w1 = (const u16*)d_in[8]; p.w3 = (const u16*)d_in[9];
  p.w2 = (const u16*)d_in[10]; p.gw = (const u16*)d_in[11]; p.out = (u16*)d_out;
  p.base = (char*)(((uintptr_t)d_ws + 2 * MB - 1) & ~(uintptr_t)(2 * MB - 1));
  p.d1 = 12; p.d2 = 6; p.mask = 63; p.pad = 0;
  return p;
}

static void launch(const Params& p, hipStream_t stream) {
  (void)hipFuncSetAttribute((const void*)mega, hipFuncAttributeMaxDynamicSharedMemorySize, LDS_BYTES);
  (void)hipMemsetAsync(p.base, 0xFF, INIT_BYTES, stream);
  mega<<<256, 256, LDS_BYTES, stream>>>(p);
}

extern "C" void kernel_launch(void* const* d_in, const int* in_sizes, int n_in, void* d_out, int out_size, void* d_ws, size_t ws_size,
                              hipStream_t stream) {
  launch(make_params(d_in, d_out, d_ws), stream);
}
```
